```python
import torch

def _softmax(x, axis=-1):
    return torch.softmax(x, dim=axis)
def _gelu(x):
    return torch.nn.functional.gelu(x, approximate="tanh")
def _silu(x):
    return torch.nn.functional.silu(x)
def _sigmoid(x):
    return torch.sigmoid(x)
def _layernorm(x, axis=-1, eps=1e-5):
    m = x.mean(dim=axis, keepdim=True)
    v = x.var(dim=axis, keepdim=True, unbiased=False)
    return (x - m) / torch.sqrt(v + eps)
def _scatter_add(tgt, idx, src, axis=0):
    return tgt.clone().index_add_(axis, idx, src)
def _segment_sum(src, seg, n):
    out = torch.zeros((n,) + src.shape[1:], dtype=src.dtype)
    return out.index_add_(0, seg, src)
def _pad(x, pw):
    flat = tuple(v for pair in reversed(pw) for v in pair)
    return torch.nn.functional.pad(x, flat)

kernel_name = 'sequential_preLN__grouped_kv_mqa__glu_gated_swiglu__prompt'

def setup_inputs():

    _w = (lambda a: (lambda t: t.to(torch.bfloat16) if t.is_floating_point() else t)(torch.as_tensor(a)))
    def _A_setup():
        B, S, D, H = 1, 2048, 512, 32
        Dh = D // H
        return {
            "x": _w((lambda *s: torch.randn(*s, dtype=torch.bfloat16) * 0.05)(B, S, D)),
            "wq": _w((lambda *s: torch.randn(*s, dtype=torch.bfloat16) * 0.05)(D, H * Dh)),
            "wk": _w((lambda *s: torch.randn(*s, dtype=torch.bfloat16) * 0.05)(D, Dh)),
            "wv": _w((lambda *s: torch.randn(*s, dtype=torch.bfloat16) * 0.05)(D, Dh)),
            "wo": _w((lambda *s: torch.randn(*s, dtype=torch.bfloat16) * 0.05)(H * Dh, D)),
        }
    _A_in = _A_setup()

    def _B_setup():
        B, S, D, F = 1, 2048, 512, 1376
        return {
            "x": _w((lambda *s: torch.randn(*s, dtype=torch.bfloat16) * 0.05)(B, S, D)),
            "w1": _w((lambda *s: torch.randn(*s, dtype=torch.bfloat16) * 0.4)(D, F) * (2.0 / D) ** 0.5),
            "w3": _w((lambda *s: torch.randn(*s, dtype=torch.bfloat16) * 0.4)(D, F) * (2.0 / D) ** 0.5),
            "w2": _w((lambda *s: torch.randn(*s, dtype=torch.bfloat16) * 0.4)(F, D) * (2.0 / F) ** 0.5),
        }
    _B_in = _B_setup()

    return {
        "A_x": _A_in["x"],
        "A_wq": _A_in["wq"],
        "A_wk": _A_in["wk"],
        "A_wv": _A_in["wv"],
        "A_wo": _A_in["wo"],
        "B_x": _B_in["x"],
        "B_w1": _B_in["w1"],
        "B_w3": _B_in["w3"],
        "B_w2": _B_in["w2"],
    }

def reference(A_x, A_wq, A_wk, A_wv, A_wo, B_x, B_w1, B_w3, B_w2):
    def _A_ref(x, wq, wk, wv, wo):
        B, S, D = x.shape
        H = 32
        Dh = D // H
        q = (lambda x, *d: x.permute(*d))((x @ wq).reshape(B, S, H, Dh), 0, 2, 1, 3)
        k = x @ wk
        v = x @ wv
        scores = torch.einsum("bhqd,bkd->bhqk", q, k) * (Dh ** -0.5)
        p = _softmax(scores, axis=-1)
        o = torch.einsum("bhqk,bkd->bhqd", p, v)
        return (lambda x, *d: x.permute(*d))(o, 0, 2, 1, 3).reshape(B, S, H * Dh) @ wo

    def _B_ref(x, w1, w3, w2):
        gate = torch.einsum("bsd,df->bsf", x, w1)
        up = torch.einsum("bsd,df->bsf", x, w3)
        h = _silu(gate) * up
        out = torch.einsum("bsf,fd->bsd", h, w2)
        return out

    x = A_x
    h = x + _A_ref(_layernorm(x, axis=-1), A_wq, A_wk, A_wv, A_wo)
    h2 = h + _B_ref(_layernorm(h, axis=-1), B_w1, B_w3, B_w2)
    return h2
```

```cpp
#include <hip/hip_runtime.h>
#include <hip/hip_bf16.h>
#include <stdint.h>


typedef short bf16x8 __attribute__((ext_vector_type(8)));
typedef short s16x4 __attribute__((ext_vector_type(4)));
typedef float f32x16 __attribute__((ext_vector_type(16)));
typedef float f32x2 __attribute__((ext_vector_type(2)));
typedef __bf16 bf16x2v __attribute__((ext_vector_type(2)));
typedef unsigned u32x4 __attribute__((ext_vector_type(4)));
typedef unsigned u32x2 __attribute__((ext_vector_type(2)));
typedef __attribute__((address_space(3))) s16x4 lds_s16x4;
typedef unsigned short u16;

#define DI __device__ __forceinline__
#define MFMA32(a, b, c) __builtin_amdgcn_mfma_f32_32x32x16_bf16((a), (b), (c), 0, 0, 0)

constexpr int S_ = 2048, D_ = 512, F_ = 1376;

DI unsigned pk2(float a, float b) {
  f32x2 v = {a, b};
  bf16x2v r = __builtin_convertvector(v, bf16x2v);
  return __builtin_bit_cast(unsigned, r);
}
DI float bflo(unsigned u) { return __uint_as_float(u << 16); }
DI float bfhi(unsigned u) { return __uint_as_float(u & 0xffff0000u); }

DI unsigned woff(unsigned row, unsigned ch) {
  return 256u * row + 16u * (ch ^ (((row & 3) << 2) | ((row >> 2) & 3)));
}
DI unsigned tr_addr(unsigned lane, unsigned c, unsigned ks, unsigned t) {
  const unsigned h = lane >> 5, blk = (lane >> 4) & 1, q = (lane & 15) >> 2, p = lane & 3;
  return woff(16 * ks + 8 * h + 4 * t + q, 4 * c + 2 * blk + (p >> 1)) + 8 * (p & 1);
}

constexpr int WCHUNK_BYTES = 128 * 256;

template <int NT>
DI void gemm_chunk(f32x16 (&acc)[NT], const char* Xs, int xstride, int kchunk8, const char* Ws,
                   int mrow, int c0, int lane) {
  const int h = lane >> 5;
#pragma unroll
  for (int ks = 0; ks < 8; ++ks) {
    bf16x8 xb = *(const bf16x8*)(Xs + mrow * xstride + (((kchunk8 + 2 * ks + h) ^ (mrow & 15)) << 4));
#pragma unroll
    for (int t = 0; t < NT; ++t) {
      s16x4 lo = __builtin_amdgcn_ds_read_tr16_b64_v4i16((lds_s16x4*)(Ws + tr_addr(lane, c0 + t, ks, 0)));
      s16x4 hi = __builtin_amdgcn_ds_read_tr16_b64_v4i16((lds_s16x4*)(Ws + tr_addr(lane, c0 + t, ks, 1)));
      bf16x8 wa = __builtin_shufflevector(lo, hi, 0, 1, 2, 3, 4, 5, 6, 7);
      acc[t] = MFMA32(wa, xb, acc[t]);
    }
  }
}

DI float wave_sum(float v) {
#pragma unroll
  for (int o = 32; o > 0; o >>= 1) v += __shfl_xor(v, o);
  return v;
}

template <int NR>
DI void ln_rows_to_lds(const u16* __restrict__ src, int row0_global, int row0_tile, char* Xs, int lane) {
  u32x4 v[NR];
#pragma unroll
  for (int i = 0; i < NR; ++i) v[i] = *(const u32x4*)(src + (size_t)(row0_global + i) * D_ + lane * 8);
#pragma unroll
  for (int i = 0; i < NR; ++i) {
    float f[8];
#pragma unroll
    for (int j = 0; j < 4; ++j) { f[2 * j] = bflo(v[i][j]); f[2 * j + 1] = bfhi(v[i][j]); }
    float s = 0.f;
#pragma unroll
    for (int j = 0; j < 8; ++j) s += f[j];
    s = wave_sum(s);
    const float mean = s * (1.f / D_);
    float q = 0.f;
#pragma unroll
    for (int j = 0; j < 8; ++j) { f[j] -= mean; q += f[j] * f[j]; }
    q = wave_sum(q);
    const float rstd = 1.f / sqrtf(q * (1.f / D_) + 1e-5f);
    u32x4 o;
#pragma unroll
    for (int j = 0; j < 4; ++j) o[j] = pk2(f[2 * j] * rstd, f[2 * j + 1] * rstd);
    const int r = row0_tile + i;
    *(u32x4*)(Xs + r * 1024 + ((lane ^ (r & 15)) << 4)) = o;
  }
}

__global__ __launch_bounds__(256) void qkv_kernel(const u16* __restrict__ x, const u16* __restrict__ wq,
                                                  const u16* __restrict__ wk, const u16* __restrict__ wv,
                                                  u16* __restrict__ Q, u16* __restrict__ Kf, u16* __restrict__ Vf) {
  __shared__ __attribute__((aligned(16))) char sm[65536 + 2 * WCHUNK_BYTES];
  char* Xs = sm;
  char* Ws = sm + 65536;
  const int tid = threadIdx.x, lane = tid & 63, wid = tid >> 6;
  const int row0 = blockIdx.x * 64, cb = blockIdx.y;
  const int ch = tid & 15, wr = tid >> 4;
  const u16* wsrc;
  int wld;
  bool wvalid = true;
  if (cb < 4) { wsrc = wq + cb * 128 + ch * 8; wld = 512; }
  else if (ch < 2) { wsrc = wk + ch * 8; wld = 16; }
  else if (ch < 4) { wsrc = wv + (ch - 2) * 8; wld = 16; }
  else { wsrc = wq; wld = 0; wvalid = false; }
  u32x4 wreg[8];
  auto wload = [&](int k0) {
#pragma unroll
    for (int i = 0; i < 8; ++i) {
      u32x4 z = {0, 0, 0, 0};
      wreg[i] = wvalid ? *(const u32x4*)(wsrc + (size_t)(k0 + wr + 16 * i) * wld) : z;
    }
  };
  auto wstore = [&](char* dst) {
#pragma unroll
    for (int i = 0; i < 8; ++i) *(u32x4*)(dst + woff(wr + 16 * i, ch)) = wreg[i];
  };
  wload(0);
  ln_rows_to_lds<16>(x, row0 + wid * 16, wid * 16, Xs, lane);
  wstore(Ws);
  __syncthreads();
  const int wm = wid & 1, wn = wid >> 1;
  const int mrow = wm * 32 + (lane & 31);
  f32x16 acc[2] = {};
  for (int c = 0; c < 4; ++c) {
    if (c + 1 < 4) wload((c + 1) * 128);
    gemm_chunk<2>(acc, Xs, 1024, c * 16, Ws + (c & 1) * WCHUNK_BYTES, mrow, wn * 2, lane);
    if (c + 1 < 4) wstore(Ws + ((c + 1) & 1) * WCHUNK_BYTES);
    __syncthreads();
  }
  const int h = lane >> 5;
  const int m = row0 + mrow;
  if (cb < 4) {
    const float qs = 0.25f * 1.4426950408889634f;
#pragma unroll
    for (int t = 0; t < 2; ++t) {
#pragma unroll
      for (int g = 0; g < 4; ++g) {
        u32x2 o = {pk2(acc[t][4 * g] * qs, acc[t][4 * g + 1] * qs), pk2(acc[t][4 * g + 2] * qs, acc[t][4 * g + 3] * qs)};
        *(u32x2*)(Q + (size_t)m * D_ + cb * 128 + (wn * 2 + t) * 32 + 8 * g + 4 * h) = o;
      }
    }
  } else if (wn == 0) {
#pragma unroll
    for (int g = 0; g < 2; ++g) {
      u32x2 o = {pk2(acc[0][4 * g], acc[0][4 * g + 1]), pk2(acc[0][4 * g + 2], acc[0][4 * g + 3])};
      *(u32x2*)(Kf + (size_t)m * 16 + 8 * g + 4 * h) = o;
    }
    const int kt = m >> 5, ks = (m >> 4) & 1, ka = (m >> 3) & 1, kh = (m >> 2) & 1, kb = m & 3;
    u16* vbase = Vf + (size_t)((((kt * 2 + ks) * 2 + kh) * 16) * 8) + 4 * ka + kb;
#pragma unroll
    for (int g = 2; g < 4; ++g) {
#pragma unroll
      for (int e = 0; e < 4; ++e) {
        const int d = 8 * (g - 2) + 4 * h + e;
        vbase[d * 8] = (u16)(pk2(acc[0][4 * g + e], 0.f) & 0xffffu);
      }
    }
  }
}

constexpr int ATT_LDS = 131072 + 64;
__global__ __launch_bounds__(512) void attn_kernel(const u16* __restrict__ Q, const u16* __restrict__ Kf,
                                                   const u16* __restrict__ Vf, u16* __restrict__ O) {
  __shared__ __attribute__((aligned(16))) char sm[ATT_LDS];
  const int tid = threadIdx.x, lane = tid & 63, wid = tid >> 6;
  {
    u32x4 t0[8], t1[8];
#pragma unroll
    for (int i = 0; i < 8; ++i) t0[i] = *(const u32x4*)((const char*)Kf + (size_t)(tid + 512 * i) * 16);
#pragma unroll
    for (int i = 0; i < 8; ++i) t1[i] = *(const u32x4*)((const char*)Vf + (size_t)(tid + 512 * i) * 16);
#pragma unroll
    for (int i = 0; i < 8; ++i) *(u32x4*)(sm + (tid + 512 * i) * 16) = t0[i];
#pragma unroll
    for (int i = 0; i < 8; ++i) *(u32x4*)(sm + 65536 + (tid + 512 * i) * 16) = t1[i];
    if (tid < 8) ((unsigned*)(sm + 131072))[tid] = tid < 4 ? 0x3f803f80u : 0u;
  }
  const int h = lane >> 5, r = lane & 31;
  const int q = blockIdx.x * 8 + wid;
  const bf16x8 qf = *(const bf16x8*)(Q + (size_t)q * D_ + r * 16 + h * 8);
  __syncthreads();
  const char* kp = sm + r * 32 + h * 16;
  const char* vp0 = sm + (r < 16 ? 65536 + h * 256 + r * 16 : (r == 16 ? 131072 : 131088));
  const char* vp1 = vp0 + (r < 16 ? 512 : 0);
  const int vstride = r < 16 ? 1024 : 0;
  f32x16 negm;
  {
    f32x16 z = {};
    f32x16 s0 = MFMA32(*(const bf16x8*)kp, qf, z);
    float mx = s0[0];
#pragma unroll
    for (int i = 1; i < 16; ++i) mx = fmaxf(mx, s0[i]);
    mx = fmaxf(mx, __shfl_xor(mx, 32));
#pragma unroll
    for (int i = 0; i < 16; ++i) negm[i] = -mx;
  }
  f32x16 o = {};
  for (int t = 0; t < 64; ++t) {
    const bf16x8 kf = *(const bf16x8*)(kp + t * 1024);
    const bf16x8 v0 = *(const bf16x8*)(vp0 + t * vstride);
    const bf16x8 v1 = *(const bf16x8*)(vp1 + t * vstride);
    f32x16 s = MFMA32(kf, qf, negm);
    float mx = s[0];
#pragma unroll
    for (int i = 1; i < 16; ++i) mx = fmaxf(mx, s[i]);
    if (__builtin_expect(__any(mx > 8.f), 0)) {
      mx = fmaxf(mx, __shfl_xor(mx, 32));
      mx = fmaxf(mx, 0.f);
      const float sc = __builtin_amdgcn_exp2f(-mx);
#pragma unroll
      for (int i = 0; i < 16; ++i) { o[i] *= sc; negm[i] -= mx; s[i] -= mx; }
    }
#pragma unroll
    for (int i = 0; i < 16; ++i) s[i] = __builtin_amdgcn_exp2f(s[i]);
    u32x4 p0, p1;
#pragma unroll
    for (int i = 0; i < 4; ++i) { p0[i] = pk2(s[2 * i], s[2 * i + 1]); p1[i] = pk2(s[8 + 2 * i], s[9 + 2 * i]); }
    o = MFMA32(v0, __builtin_bit_cast(bf16x8, p0), o);
    o = MFMA32(v1, __builtin_bit_cast(bf16x8, p1), o);
  }
  const float l = __shfl(o[8], r);
  const float rl = 1.f / l;
  u32x2 oa = {pk2(o[0] * rl, o[1] * rl), pk2(o[2] * rl, o[3] * rl)};
  u32x2 ob = {pk2(o[4] * rl, o[5] * rl), pk2(o[6] * rl, o[7] * rl)};
  *(u32x2*)(O + (size_t)q * D_ + r * 16 + 4 * h) = oa;
  *(u32x2*)(O + (size_t)q * D_ + r * 16 + 8 + 4 * h) = ob;
}

template <int KTOT>
__global__ __launch_bounds__(256) void gemm_res_kernel(const u16* __restrict__ A, const u16* __restrict__ W,
                                                       const u16* __restrict__ res, u16* __restrict__ out) {
  constexpr int KCH = (KTOT / 8 + 15) / 16 * 16;
  constexpr int XSTR = KCH * 16;
  constexpr int NC = (KTOT + 127) / 128;
  __shared__ __attribute__((aligned(16))) char sm[32 * XSTR + 2 * WCHUNK_BYTES];
  char* Xs = sm;
  char* Ws = sm + 32 * XSTR;
  const int tid = threadIdx.x, lane = tid & 63, wid = tid >> 6;
  const int row0 = blockIdx.x * 32, n0 = blockIdx.y * 128;
  const int ch = tid & 15, wr = tid >> 4;
  const u16* wsrc = W + n0 + ch * 8;
  u32x4 wreg[8];
  auto wload = [&](int k0) {
#pragma unroll
    for (int i = 0; i < 8; ++i) {
      u32x4 z = {0, 0, 0, 0};
      const int k = k0 + wr + 16 * i;
      wreg[i] = (k < KTOT) ? *(const u32x4*)(wsrc + (size_t)k * 512) : z;
    }
  };
  auto wstore = [&](char* dst) {
#pragma unroll
    for (int i = 0; i < 8; ++i) *(u32x4*)(dst + woff(wr + 16 * i, ch)) = wreg[i];
  };
  wload(0);
  {
    constexpr int TOT = 32 * KCH;
    constexpr int PER = (TOT + 255) / 256;
    u32x4 a[PER];
#pragma unroll
    for (int i = 0; i < PER; ++i) {
      const int idx = tid + 256 * i;
      const int rr = idx / KCH, cc = idx % KCH;
      u32x4 z = {0, 0, 0, 0};
      a[i] = (idx < TOT && cc < KTOT / 8) ? *(const u32x4*)(A + (size_t)(row0 + rr) * KTOT + cc * 8) : z;
    }
#pragma unroll
    for (int i = 0; i < PER; ++i) {
      const int idx = tid + 256 * i;
      const int rr = idx / KCH, cc = idx % KCH;
      if (idx < TOT) *(u32x4*)(Xs + rr * XSTR + ((cc ^ (rr & 15)) << 4)) = a[i];
    }
  }
  wstore(Ws);
  __syncthreads();
  const int mrow = lane & 31;
  f32x16 acc[1] = {};
  for (int c = 0; c < NC; ++c) {
    if (c + 1 < NC) wload((c + 1) * 128);
    gemm_chunk<1>(acc, Xs, XSTR, c * 16, Ws + (c & 1) * WCHUNK_BYTES, mrow, wid, lane);
    if (c + 1 < NC) wstore(Ws + ((c + 1) & 1) * WCHUNK_BYTES);
    __syncthreads();
  }
  const int h = lane >> 5;
  const size_t base = (size_t)(row0 + mrow) * D_ + n0 + wid * 32 + 4 * h;
#pragma unroll
  for (int g = 0; g < 4; ++g) {
    u32x2 rv = *(const u32x2*)(res + base + 8 * g);
    u32x2 o = {pk2(acc[0][4 * g] + bflo(rv[0]), acc[0][4 * g + 1] + bfhi(rv[0])),
               pk2(acc[0][4 * g + 2] + bflo(rv[1]), acc[0][4 * g + 3] + bfhi(rv[1]))};
    *(u32x2*)(out + base + 8 * g) = o;
  }
}

__global__ __launch_bounds__(256) void ffn1_kernel(const u16* __restrict__ hsrc, const u16* __restrict__ w1,
                                                   const u16* __restrict__ w3, u16* __restrict__ act) {
  __shared__ __attribute__((aligned(16))) char sm[65536 + 2 * WCHUNK_BYTES];
  char* Xs = sm;
  char* Ws = sm + 65536;
  const int tid = threadIdx.x, lane = tid & 63, wid = tid >> 6;
  const int row0 = blockIdx.x * 64, f0 = blockIdx.y * 64;
  const int ch = tid & 15, wr = tid >> 4;
  const int c32 = ch >> 2;
  const int fcol = f0 + (c32 >> 1) * 32 + (ch & 3) * 8;
  const bool wvalid = fcol < F_;
  const u16* wsrc = ((c32 & 1) ? w3 : w1) + fcol;
  u32x4 wreg[8];
  auto wload = [&](int k0) {
#pragma unroll
    for (int i = 0; i < 8; ++i) {
      u32x4 z = {0, 0, 0, 0};
      wreg[i] = wvalid ? *(const u32x4*)(wsrc + (size_t)(k0 + wr + 16 * i) * F_) : z;
    }
  };
  auto wstore = [&](char* dst) {
#pragma unroll
    for (int i = 0; i < 8; ++i) *(u32x4*)(dst + woff(wr + 16 * i, ch)) = wreg[i];
  };
  wload(0);
  ln_rows_to_lds<16>(hsrc, row0 + wid * 16, wid * 16, Xs, lane);
  wstore(Ws);
  __syncthreads();
  const int wm = wid & 1, wn = wid >> 1;
  const int mrow = wm * 32 + (lane & 31);
  f32x16 acc[2] = {};
  for (int c = 0; c < 4; ++c) {
    if (c + 1 < 4) wload((c + 1) * 128);
    gemm_chunk<2>(acc, Xs, 1024, c * 16, Ws + (c & 1) * WCHUNK_BYTES, mrow, wn * 2, lane);
    if (c + 1 < 4) wstore(Ws + ((c + 1) & 1) * WCHUNK_BYTES);
    __syncthreads();
  }
  const int h = lane >> 5;
  const int fb = f0 + wn * 32;
  if (fb < F_) {
    const size_t base = (size_t)(row0 + mrow) * F_ + fb + 4 * h;
#pragma unroll
    for (int g = 0; g < 4; ++g) {
      float r[4];
#pragma unroll
      for (int e = 0; e < 4; ++e) {
        const float gt = bflo(pk2(acc[0][4 * g + e], 0.f));
        const float up = bflo(pk2(acc[1][4 * g + e], 0.f));
        const float sg = gt / (1.f + __expf(-gt));
        r[e] = bflo(pk2(sg, 0.f)) * up;
      }
      u32x2 o = {pk2(r[0], r[1]), pk2(r[2], r[3])};
      *(u32x2*)(act + base + 8 * g) = o;
    }
  }
}

extern "C" void kernel_launch(void* const* d_in, const int* in_sizes, int n_in, void* d_out, int out_size,
                              void* d_ws, size_t ws_size, hipStream_t stream) {
  const u16* x = (const u16*)d_in[0];
  const u16* wq = (const u16*)d_in[1];
  const u16* wk = (const u16*)d_in[2];
  const u16* wv = (const u16*)d_in[3];
  const u16* wo = (const u16*)d_in[4];
  const u16* w1 = (const u16*)d_in[6];
  const u16* w3 = (const u16*)d_in[7];
  const u16* w2 = (const u16*)d_in[8];
  u16* out = (u16*)d_out;
  char* ws = (char*)d_ws;
  u16* Q = (u16*)(ws);
  u16* Kf = (u16*)(ws + (2u << 20));
  u16* Vf = (u16*)(ws + (2u << 20) + 65536);
  u16* O = (u16*)(ws + (3u << 20));
  u16* Hb = (u16*)(ws + (5u << 20));
  u16* ACT = (u16*)(ws + (7u << 20));
  qkv_kernel<<<dim3(32, 5), 256, 0, stream>>>(x, wq, wk, wv, Q, Kf, Vf);
  attn_kernel<<<dim3(256), 512, 0, stream>>>(Q, Kf, Vf, O);
  gemm_res_kernel<512><<<dim3(64, 4), 256, 0, stream>>>(O, wo, x, Hb);
  ffn1_kernel<<<dim3(32, 22), 256, 0, stream>>>(Hb, w1, w3, ACT);
  gemm_res_kernel<1376><<<dim3(64, 4), 256, 0, stream>>>(ACT, w2, Hb, out);
}
```

```cpp
#include <hip/hip_runtime.h>
#include <hip/hip_bf16.h>
#include <stdint.h>


typedef short bf16x8 __attribute__((ext_vector_type(8)));
typedef short s16x4 __attribute__((ext_vector_type(4)));
typedef float f32x16 __attribute__((ext_vector_type(16)));
typedef float f32x4 __attribute__((ext_vector_type(4)));
typedef float f32x2 __attribute__((ext_vector_type(2)));
typedef __bf16 bf16x2v __attribute__((ext_vector_type(2)));
typedef unsigned u32x4 __attribute__((ext_vector_type(4)));
typedef unsigned u32x2 __attribute__((ext_vector_type(2)));
typedef __attribute__((address_space(3))) s16x4 lds_s16x4;
typedef unsigned short u16;

#define DI __device__ __forceinline__
#define MFMA32(a, b, c) __builtin_amdgcn_mfma_f32_32x32x16_bf16((a), (b), (c), 0, 0, 0)

constexpr int S_ = 2048, D_ = 512, F_ = 1376;

DI unsigned pk2(float a, float b) {
  f32x2 v = {a, b};
  bf16x2v r = __builtin_convertvector(v, bf16x2v);
  return __builtin_bit_cast(unsigned, r);
}
DI float bflo(unsigned u) { return __uint_as_float(u << 16); }
DI float bfhi(unsigned u) { return __uint_as_float(u & 0xffff0000u); }
DI float dot2bf(unsigned a, unsigned b, float c) {
  return __builtin_amdgcn_fdot2_f32_bf16(__builtin_bit_cast(bf16x2v, a), __builtin_bit_cast(bf16x2v, b), c, false);
}

DI void glds16(const void* gsrc, unsigned lds_dst) {
  unsigned keep;
  asm volatile("s_mov_b32 %0, m0\n\ts_mov_b32 m0, %2\n\ts_nop 0\n\tglobal_load_lds_dwordx4 %1, off\n\ts_mov_b32 m0, %0"
               : "=&s"(keep) : "v"(gsrc), "s"(lds_dst) : "memory");
}

enum { MODE_QKV = 0, MODE_RES = 1, MODE_FFN1 = 2 };

template <int BN>
DI unsigned wswz(unsigned row) {
  if constexpr (BN == 128) return ((row & 3) << 2) | ((row >> 2) & 3);
  else return ((row >> 1) & 1) << 2;
}

struct GemmArgs {
  const u16* A;
  const u16* W0;
  const u16* W1;
  const u16* W2;
  const u16* res;
  u16* out0;
  u16* out1;
  u16* out2;
  float* hstat;
  float* csum13;
  float* kmax;
  const u16* w1;
  const u16* w3;
};

template <int MODE, int BM, int BN, int KTOT, int NST, int DBG = 0>
__global__ __launch_bounds__(256) void gemm_kernel(GemmArgs args) {
  constexpr int WMT = BM / 64, WNT = BN / 64;
  constexpr int XB = BM * 128, WB = 64 * BN * 2, STB = XB + WB;
  constexpr int NXI = BM / 32, NWI = BN / 32, G = NXI + NWI;
  constexpr int NKT = (KTOT + 63) / 64;
  constexpr bool LN = (MODE != MODE_RES);
  constexpr bool LNSTAT = (MODE == MODE_QKV);
  constexpr int LDA = (MODE == MODE_RES) ? KTOT : 512;
  constexpr int LDW = (MODE == MODE_FFN1) ? F_ : 512;
  __shared__ __attribute__((aligned(16))) char sm[NST * STB];
  const int tid = threadIdx.x, lane = tid & 63, wid = tid >> 6;
  const int wm = wid & 1, wn = wid >> 1, h = lane >> 5;
  const int row0 = blockIdx.x * BM, cb = blockIdx.y;
  if constexpr (MODE == MODE_QKV) {
    if (cb == 9) {
      const int j = blockIdx.x;
      if (tid < 128) args.hstat[j * 128 + tid] = 0.f;
      const u16* wsrc = (j < 16 ? args.w1 : args.w3) + (j & 15) * 86;
      float* red = (float*)sm;
      const int cp = tid % 43, rg = tid / 43;
      float a0 = 0.f, a1 = 0.f;
      if (rg < 5) {
        const int r0 = rg * 103, r1 = min(r0 + 103, 512);
#pragma unroll 8
        for (int r = r0; r < r1; ++r) {
          const unsigned u = *(const unsigned*)(wsrc + (size_t)r * F_ + cp * 2);
          a0 += bflo(u); a1 += bfhi(u);
        }
        red[(rg * 43 + cp) * 2] = a0; red[(rg * 43 + cp) * 2 + 1] = a1;
      }
      __syncthreads();
      if (tid < 86) {
        float t = 0.f;
#pragma unroll
        for (int g = 0; g < 5; ++g) t += red[g * 86 + tid];
        args.csum13[(j < 16 ? 0 : F_) + (j & 15) * 86 + tid] = t;
      }
      return;
    }
  }

  const u16* xp[NXI];
#pragma unroll
  for (int i = 0; i < NXI; ++i) {
    const int r = (i * 4 + wid) * 8 + (lane >> 3), c = (lane & 7) ^ ((r >> 1) & 7);
    xp[i] = args.A + (size_t)(row0 + r) * LDA + c * 8;
  }
  const u16* wp[NWI];
  int wrow[NWI], wld[NWI];
#pragma unroll
  for (int i = 0; i < NWI; ++i) {
    const int p = (i * 4 + wid) * 64 + lane;
    const int r = p / (BN / 8), c = (p % (BN / 8)) ^ wswz<BN>(r);
    wrow[i] = r;
    if constexpr (MODE == MODE_QKV) {
      if (cb < 8) { wp[i] = args.W0 + cb * 64 + c * 8; wld[i] = 512; }
      else if (c < 2) { wp[i] = args.W1 + c * 8; wld[i] = 16; }
      else { wp[i] = args.W2 + (c & 1) * 8; wld[i] = 16; }
    } else if constexpr (MODE == MODE_RES) {
      wp[i] = args.W0 + cb * BN + c * 8; wld[i] = 512;
    } else {
      const int c32 = c >> 2;
      int f = cb * 64 + (c32 >> 1) * 32 + (c & 3) * 8;
      if (f >= F_) f = 0;
      wp[i] = ((c32 & 1) ? args.W1 : args.W0) + f; wld[i] = F_;
    }
  }
  const unsigned lds0 = (unsigned)(size_t)sm + (unsigned)__builtin_amdgcn_readfirstlane(wid) * 1024u;
  auto issue = [&](int kt, int buf) {
    const unsigned base = lds0 + buf * STB;
#pragma unroll
    for (int i = 0; i < NXI; ++i) glds16(xp[i] + kt * 64, base + i * 4096);
#pragma unroll
    for (int i = 0; i < NWI; ++i) {
      int k = kt * 64 + wrow[i];
      if constexpr (KTOT % 64 != 0) k = k < KTOT ? k : KTOT - 1;
      glds16(wp[i] + (size_t)k * wld[i], base + XB + i * 4096);
    }
  };

  f32x16 acc[WNT][WMT] = {};
  float sx = 0.f, sxx = 0.f, cs = 0.f;
  const int wmu = __builtin_amdgcn_readfirstlane(wm), wnu = __builtin_amdgcn_readfirstlane(wn);
  unsigned xoff[WMT];
#pragma unroll
  for (int t = 0; t < WMT; ++t) xoff[t] = (unsigned)((wm * WMT + t) * 32 + (lane & 31)) * 128u;
  const unsigned xsw = ((lane & 31) >> 1) & 7;
  const unsigned tq = (lane & 15) >> 2, tp = lane & 3, tblk = (lane >> 4) & 1;

  constexpr int PD = NST - 1;
#pragma unroll
  for (int i = 0; i < PD; ++i)
    if (i < NKT) issue(i, i);
  for (int kt = 0; kt < NKT; ++kt) {
    const int rem = NKT - 1 - kt;
    if (rem >= PD - 1) asm volatile("s_waitcnt vmcnt(%0)" ::"n"((PD - 1) * G) : "memory");
    else if (PD >= 3 && rem == 1) asm volatile("s_waitcnt vmcnt(%0)" ::"n"(G) : "memory");
    else if (PD >= 4 && rem == 2) asm volatile("s_waitcnt vmcnt(%0)" ::"n"(2 * G) : "memory");
    else asm volatile("s_waitcnt vmcnt(0)" ::: "memory");
    __builtin_amdgcn_s_barrier();
    asm volatile("" ::: "memory");
    if (kt + PD < NKT && DBG != 2) issue(kt + PD, (kt + PD) % NST);
    const char* Xs = sm + (kt % NST) * STB;
    const char* Ws = Xs + XB;
    const int nks = (KTOT % 64 != 0 && kt == NKT - 1) ? (KTOT % 64) / 16 : 4;
#pragma unroll
    for (int ks = 0; ks < 4; ++ks) {
      if (ks < nks && DBG != 1) {
        bf16x8 xb[WMT], wa[WNT];
#pragma unroll
        for (int t = 0; t < WMT; ++t) xb[t] = *(const bf16x8*)(Xs + xoff[t] + (((2 * ks + h) ^ xsw) << 4));
#pragma unroll
        for (int t = 0; t < WNT; ++t) {
          const unsigned c = (wn * WNT + t) * 4 + 2 * tblk + (tp >> 1);
          const unsigned r0 = 16 * ks + 8 * h + tq, r1 = r0 + 4;
          s16x4 lo = __builtin_amdgcn_ds_read_tr16_b64_v4i16((lds_s16x4*)(Ws + r0 * (BN * 2) + ((c ^ wswz<BN>(r0)) << 4) + 8 * (tp & 1)));
          s16x4 hi = __builtin_amdgcn_ds_read_tr16_b64_v4i16((lds_s16x4*)(Ws + r1 * (BN * 2) + ((c ^ wswz<BN>(r1)) << 4) + 8 * (tp & 1)));
          wa[t] = __builtin_shufflevector(lo, hi, 0, 1, 2, 3, 4, 5, 6, 7);
        }
#pragma unroll
        for (int tn = 0; tn < WNT; ++tn)
#pragma unroll
          for (int tm = 0; tm < WMT; ++tm) acc[tn][tm] = MFMA32(wa[tn], xb[tm], acc[tn][tm]);
        if constexpr (LNSTAT) {
          static_assert(WMT == 1 && WNT == 1, "in-kernel LN stats assume a 64x64 tile");
          if (wmu == wnu) {
            const u32x4 u = __builtin_bit_cast(u32x4, xb[0]);
#pragma unroll
            for (int j = 0; j < 4; ++j) { sx = dot2bf(u[j], 0x3f803f80u, sx); sxx = dot2bf(u[j], u[j], sxx); }
          } else {
            const u32x4 u = __builtin_bit_cast(u32x4, wa[0]);
#pragma unroll
            for (int j = 0; j < 4; ++j) cs = dot2bf(u[j], 0x3f803f80u, cs);
          }
        }
      }
    }
  }

  float mean[WMT], rstd[WMT];
  if constexpr (LNSTAT) {
    __syncthreads();
    float* csum_s = (float*)sm;
    float* stat_s = (float*)sm + BN;
    if (wmu == wnu) {
      const float s1 = sx + __shfl_xor(sx, 32), s2 = sxx + __shfl_xor(sxx, 32);
      const float mu = s1 * (1.f / 512);
      const float var = fmaxf(s2 * (1.f / 512) - mu * mu, 0.f);
      if (h == 0) *(f32x2*)(stat_s + (wm * 32 + lane) * 2) = (f32x2){mu, 1.f / sqrtf(var + 1e-5f)};
    } else {
      const float c = cs + __shfl_xor(cs, 32);
      if (h == 0) csum_s[wn * 32 + lane] = c;
    }
    __syncthreads();
    const f32x2 st = *(const f32x2*)(stat_s + (wm * 32 + (lane & 31)) * 2);
    mean[0] = st[0]; rstd[0] = st[1];
#pragma unroll
    for (int g = 0; g < 4; ++g) {
      const f32x4 c4 = *(const f32x4*)(csum_s + wn * 32 + 8 * g + 4 * h);
#pragma unroll
      for (int e = 0; e < 4; ++e) acc[0][0][4 * g + e] = rstd[0] * (acc[0][0][4 * g + e] - mean[0] * c4[e]);
    }
  } else if constexpr (LN) {
#pragma unroll
    for (int t = 0; t < WMT; ++t) {
      const f32x2 st = *(const f32x2*)(args.hstat + (size_t)(row0 + (wm * WMT + t) * 32 + (lane & 31)) * 2);
      mean[t] = st[0] * (1.f / 512);
      const float var = fmaxf(st[1] * (1.f / 512) - mean[t] * mean[t], 0.f);
      rstd[t] = 1.f / sqrtf(var + 1e-5f);
    }
    const int fb = min(cb * 64 + wn * 32, F_ - 32);
#pragma unroll
    for (int tn = 0; tn < WNT; ++tn) {
#pragma unroll
      for (int g = 0; g < 4; ++g) {
        const f32x4 c4 = *(const f32x4*)(args.csum13 + tn * F_ + fb + 8 * g + 4 * h);
#pragma unroll
        for (int tm = 0; tm < WMT; ++tm)
#pragma unroll
          for (int e = 0; e < 4; ++e) acc[tn][tm][4 * g + e] = rstd[tm] * (acc[tn][tm][4 * g + e] - mean[tm] * c4[e]);
      }
    }
  }

#pragma unroll
  for (int tm = 0; tm < WMT; ++tm) {
    const int m = row0 + (wm * WMT + tm) * 32 + (lane & 31);
    if constexpr (MODE == MODE_QKV) {
      if (cb < 8) {
        const float qs = 0.25f * 1.4426950408889634f;
#pragma unroll
        for (int g = 0; g < 4; ++g) {
          const f32x16& a = acc[0][tm];
          u32x2 o = {pk2(a[4 * g] * qs, a[4 * g + 1] * qs), pk2(a[4 * g + 2] * qs, a[4 * g + 3] * qs)};
          *(u32x2*)(args.out0 + (size_t)m * D_ + cb * 64 + wn * 32 + 8 * g + 4 * h) = o;
        }
      } else if (wn == 0) {
        const f32x16& a = acc[0][tm];
#pragma unroll
        for (int g = 0; g < 2; ++g) {
          u32x2 o = {pk2(a[4 * g], a[4 * g + 1]), pk2(a[4 * g + 2], a[4 * g + 3])};
          *(u32x2*)(args.out1 + (size_t)m * 16 + 8 * g + 4 * h) = o;
        }
        {
          float n2 = 0.f;
#pragma unroll
          for (int i = 0; i < 8; ++i) { const float kv = bflo(pk2(a[i], 0.f)); n2 += kv * kv; }
          n2 += __shfl_xor(n2, 32);
#pragma unroll
          for (int o = 16; o > 0; o >>= 1) n2 = fmaxf(n2, __shfl_xor(n2, o));
          if (lane == 0) args.kmax[m >> 5] = sqrtf(n2) * 1.001f;
        }
        const int kt = m >> 5, ks = (m >> 4) & 1, ka = (m >> 3) & 1, kh = (m >> 2) & 1, kb = m & 3;
        u16* vbase = args.out2 + (size_t)((((kt * 2 + ks) * 2 + kh) * 16) * 8) + 4 * ka + kb;
#pragma unroll
        for (int g = 2; g < 4; ++g)
#pragma unroll
          for (int e = 0; e < 4; ++e) vbase[(8 * (g - 2) + 4 * h + e) * 8] = (u16)(pk2(a[4 * g + e], 0.f) & 0xffffu);
      }
    } else if constexpr (MODE == MODE_RES) {
      float s1 = 0.f, s2 = 0.f;
#pragma unroll
      for (int tn = 0; tn < WNT; ++tn) {
        const size_t base = (size_t)m * D_ + cb * BN + (wn * WNT + tn) * 32 + 4 * h;
#pragma unroll
        for (int g = 0; g < 4; ++g) {
          const f32x16& a = acc[tn][tm];
          const u32x2 rv = *(const u32x2*)(args.res + base + 8 * g);
          u32x2 o = {pk2(a[4 * g] + bflo(rv[0]), a[4 * g + 1] + bfhi(rv[0])),
                     pk2(a[4 * g + 2] + bflo(rv[1]), a[4 * g + 3] + bfhi(rv[1]))};
          *(u32x2*)(args.out0 + base + 8 * g) = o;
          if constexpr (KTOT == 512) {
            s1 = dot2bf(o[0], 0x3f803f80u, s1); s1 = dot2bf(o[1], 0x3f803f80u, s1);
            s2 = dot2bf(o[0], o[0], s2); s2 = dot2bf(o[1], o[1], s2);
          }
        }
      }
      if constexpr (KTOT == 512) {
        s1 += __shfl_xor(s1, 32); s2 += __shfl_xor(s2, 32);
        if (h == 0) { atomicAdd(args.hstat + (size_t)m * 2, s1); atomicAdd(args.hstat + (size_t)m * 2 + 1, s2); }
      }
    } else {
      const int fb = cb * 64 + wn * 32;
      if (fb < F_) {
        const size_t base = (size_t)m * F_ + fb + 4 * h;
#pragma unroll
        for (int g = 0; g < 4; ++g) {
          float r[4];
#pragma unroll
          for (int e = 0; e < 4; ++e) {
            const float gt = bflo(pk2(acc[0][tm][4 * g + e], 0.f));
            const float up = bflo(pk2(acc[1][tm][4 * g + e], 0.f));
            const float sg = gt * __builtin_amdgcn_rcpf(1.f + __builtin_amdgcn_exp2f(gt * -1.4426950408889634f));
            r[e] = bflo(pk2(sg, 0.f)) * up;
          }
          u32x2 o = {pk2(r[0], r[1]), pk2(r[2], r[3])};
          *(u32x2*)(args.out0 + base + 8 * g) = o;
        }
      }
    }
  }
}

constexpr int ATT_LDS = 131072 + 64;
__global__ __launch_bounds__(1024) void attn_kernel(const u16* __restrict__ Q, const u16* __restrict__ Kf,
                                                    const u16* __restrict__ Vf, const float* __restrict__ kmax,
                                                    u16* __restrict__ O) {
  __shared__ __attribute__((aligned(16))) char sm[ATT_LDS];
  const int tid = threadIdx.x, lane = tid & 63, wid = tid >> 6;
  const int widu = __builtin_amdgcn_readfirstlane(wid);
  const int h = lane >> 5, r = lane & 31;
  const int q = blockIdx.x * 8 + (wid & 7), half = wid >> 3;
  bf16x8 qf;
  asm volatile("global_load_dwordx4 %0, %1, off" : "=v"(qf) : "v"(Q + (size_t)q * D_ + r * 16 + h * 8) : "memory");
  {
    const unsigned lds0 = (unsigned)(size_t)sm;
    const int hf = (widu >> 2) & 1, isv = widu >> 3, pc = widu & 3;
    const unsigned off0 = isv * 65536 + hf * 32768 + pc * 1024;
    const char* src = (const char*)(isv ? Vf : Kf) + hf * 32768 + pc * 1024 + lane * 16;
#pragma unroll
    for (int c = 0; c < 8; ++c) glds16(src + c * 4096, lds0 + off0 + c * 4096);
    if (tid < 8) ((unsigned*)(sm + 131072))[tid] = tid < 4 ? 0x3f803f80u : 0u;
  }
  asm volatile("s_waitcnt vmcnt(8)" : "+v"(qf)::"memory");
  float qn;
  {
    const u32x4 u = __builtin_bit_cast(u32x4, qf);
    float n2 = 0.f;
#pragma unroll
    for (int j = 0; j < 4; ++j) n2 = dot2bf(u[j], u[j], n2);
    n2 += __shfl_xor(n2, 32);
    qn = sqrtf(n2) * 1.001f;
  }
  const float* kmp = kmax + __builtin_amdgcn_readfirstlane(half) * 32;
  asm volatile("s_waitcnt vmcnt(6) lgkmcnt(0)" ::: "memory");
  __builtin_amdgcn_s_barrier();
  asm volatile("" ::: "memory");
  const char* kp = sm + half * 32768 + r * 32 + h * 16;
  const char* vp0 = sm + (r < 16 ? 65536 + half * 32768 + h * 256 + r * 16 : (r == 16 ? 131072 : 131088));
  const int vd = r < 16 ? 512 : 0;
  const int vstride = r < 16 ? 1024 : 0;
  f32x16 negm, sA, sB;
  {
    f32x16 z = {};
    f32x16 s0 = MFMA32(*(const bf16x8*)kp, qf, z);
    float mx = s0[0];
#pragma unroll
    for (int i = 1; i < 16; ++i) mx = fmaxf(mx, s0[i]);
    mx = fmaxf(mx, __shfl_xor(mx, 32));
#pragma unroll
    for (int i = 0; i < 16; ++i) { negm[i] = -mx; sA[i] = s0[i] - mx; }
  }
  f32x16 o = {};
  bf16x8 kn = *(const bf16x8*)(kp + 1024);
  auto step = [&](f32x16& sc_, f32x16& sn_, int t) {
    const bf16x8 v0 = *(const bf16x8*)(vp0 + t * vstride);
    const bf16x8 v1 = *(const bf16x8*)(vp0 + t * vstride + vd);
    sn_ = MFMA32(kn, qf, negm);
    kn = *(const bf16x8*)(kp + ((t + 2) & 31) * 1024);
    if (__builtin_expect(__any(fmaf(qn, kmp[t], negm[0]) > 8.f), 0)) {
      float m0 = fmaxf(fmaxf(sc_[0], sc_[1]), sc_[2]), m1 = fmaxf(fmaxf(sc_[3], sc_[4]), sc_[5]);
      float m2 = fmaxf(fmaxf(sc_[6], sc_[7]), sc_[8]), m3 = fmaxf(fmaxf(sc_[9], sc_[10]), sc_[11]);
      float m4 = fmaxf(fmaxf(sc_[12], sc_[13]), sc_[14]);
      float mx = fmaxf(fmaxf(fmaxf(m0, m1), sc_[15]), fmaxf(fmaxf(m2, m3), m4));
      if (__any(mx > 8.f)) {
        mx = fmaxf(mx, __shfl_xor(mx, 32));
        mx = fmaxf(mx, 0.f);
        const float sc = __builtin_amdgcn_exp2f(-mx);
#pragma unroll
        for (int i = 0; i < 16; ++i) { o[i] *= sc; negm[i] -= mx; sc_[i] -= mx; sn_[i] -= mx; }
      }
    }
    u32x4 p0, p1;
#pragma unroll
    for (int i = 0; i < 4; ++i) {
      p0[i] = pk2(__builtin_amdgcn_exp2f(sc_[2 * i]), __builtin_amdgcn_exp2f(sc_[2 * i + 1]));
      p1[i] = pk2(__builtin_amdgcn_exp2f(sc_[8 + 2 * i]), __builtin_amdgcn_exp2f(sc_[9 + 2 * i]));
    }
    o = MFMA32(v0, __builtin_bit_cast(bf16x8, p0), o);
    o = MFMA32(v1, __builtin_bit_cast(bf16x8, p1), o);
  };
  for (int c = 0; c < 8; ++c) {
    if (c > 0) {
      if (c == 1) asm volatile("s_waitcnt vmcnt(5)" ::: "memory");
      else if (c == 2) asm volatile("s_waitcnt vmcnt(4)" ::: "memory");
      else if (c == 3) asm volatile("s_waitcnt vmcnt(3)" ::: "memory");
      else if (c == 4) asm volatile("s_waitcnt vmcnt(2)" ::: "memory");
      else if (c == 5) asm volatile("s_waitcnt vmcnt(1)" ::: "memory");
      else asm volatile("s_waitcnt vmcnt(0)" ::: "memory");
      if (c <= 6) {
        __builtin_amdgcn_s_barrier();
        asm volatile("" ::: "memory");
      }
    }
    step(sA, sB, 4 * c);
    step(sB, sA, 4 * c + 1);
    step(sA, sB, 4 * c + 2);
    step(sB, sA, 4 * c + 3);
  }
  __syncthreads();
  float* ex = (float*)sm + (wid & 7) * (64 * 10);
  if (half == 1) {
#pragma unroll
    for (int i = 0; i < 9; ++i) ex[i * 64 + lane] = o[i];
    ex[9 * 64 + lane] = negm[0];
  }
  __syncthreads();
  if (half == 0) {
    const float nm1 = ex[9 * 64 + lane], nm0 = negm[0];
    const float nm = fminf(nm0, nm1);
    const float a0 = __builtin_amdgcn_exp2f(nm - nm0), a1 = __builtin_amdgcn_exp2f(nm - nm1);
#pragma unroll
    for (int i = 0; i < 9; ++i) o[i] = o[i] * a0 + ex[i * 64 + lane] * a1;
    const float l = __shfl(o[8], r);
    const float rl = 1.f / l;
    u32x2 oa = {pk2(o[0] * rl, o[1] * rl), pk2(o[2] * rl, o[3] * rl)};
    u32x2 ob = {pk2(o[4] * rl, o[5] * rl), pk2(o[6] * rl, o[7] * rl)};
    *(u32x2*)(O + (size_t)q * D_ + r * 16 + 4 * h) = oa;
    *(u32x2*)(O + (size_t)q * D_ + r * 16 + 8 + 4 * h) = ob;
  }
}

extern "C" void kernel_launch(void* const* d_in, const int* in_sizes, int n_in, void* d_out, int out_size,
                              void* d_ws, size_t ws_size, hipStream_t stream) {
  const u16* x = (const u16*)d_in[0];
  const u16* wq = (const u16*)d_in[1];
  const u16* wk = (const u16*)d_in[2];
  const u16* wv = (const u16*)d_in[3];
  const u16* wo = (const u16*)d_in[4];
  const u16* w1 = (const u16*)d_in[6];
  const u16* w3 = (const u16*)d_in[7];
  const u16* w2 = (const u16*)d_in[8];
  u16* out = (u16*)d_out;
  char* ws = (char*)d_ws;
  u16* Q = (u16*)(ws);
  u16* Kf = (u16*)(ws + (2u << 20));
  u16* Vf = (u16*)(ws + (2u << 20) + 65536);
  u16* O = (u16*)(ws + (3u << 20));
  u16* Hb = (u16*)(ws + (5u << 20));
  u16* ACT = (u16*)(ws + (7u << 20));
  float* hstat = (float*)(ws + (2u << 20) + 131072);
  float* csum13 = hstat + 4096;
  float* kmax = csum13 + 2 * F_;
  {
    GemmArgs a = {x, wq, wk, wv, nullptr, Q, Kf, Vf, hstat, csum13, kmax, w1, w3};
    gemm_kernel<MODE_QKV, 64, 64, 512, 4><<<dim3(32, 10), 256, 0, stream>>>(a);
  }
  attn_kernel<<<dim3(256), 1024, 0, stream>>>(Q, Kf, Vf, kmax, O);
  {
    GemmArgs a = {O, wo, nullptr, nullptr, x, Hb, nullptr, nullptr, hstat, nullptr, nullptr, nullptr, nullptr};
    gemm_kernel<MODE_RES, 64, 64, 512, 4><<<dim3(32, 8), 256, 0, stream>>>(a);
  }
  {
    GemmArgs a = {Hb, w1, w3, nullptr, nullptr, ACT, nullptr, nullptr, hstat, csum13, nullptr, nullptr, nullptr};
    gemm_kernel<MODE_FFN1, 128, 128, 512, 2><<<dim3(16, 22), 256, 0, stream>>>(a);
  }
  {
    GemmArgs a = {ACT, w2, nullptr, nullptr, Hb, out, nullptr, nullptr, nullptr, nullptr, nullptr, nullptr, nullptr};
    gemm_kernel<MODE_RES, 64, 64, 1376, 4><<<dim3(32, 8), 256, 0, stream>>>(a);
  }
}
```

```cpp
#include <hip/hip_runtime.h>
#include <hip/hip_bf16.h>
#include <stdint.h>
#include <type_traits>


typedef short bf16x8 __attribute__((ext_vector_type(8)));
typedef short s16x4 __attribute__((ext_vector_type(4)));
typedef float f32x16 __attribute__((ext_vector_type(16)));
typedef float f32x4 __attribute__((ext_vector_type(4)));
typedef float f32x2 __attribute__((ext_vector_type(2)));
typedef __bf16 bf16x2v __attribute__((ext_vector_type(2)));
typedef unsigned u32x4 __attribute__((ext_vector_type(4)));
typedef unsigned u32x2 __attribute__((ext_vector_type(2)));
typedef __attribute__((address_space(3))) s16x4 lds_s16x4;
typedef unsigned short u16;

#define DI __device__ __forceinline__
#define MFMA32(a, b, c) __builtin_amdgcn_mfma_f32_32x32x16_bf16((a), (b), (c), 0, 0, 0)

constexpr int S_ = 2048, D_ = 512, F_ = 1376;

DI unsigned pk2(float a, float b) {
  f32x2 v = {a, b};
  bf16x2v r = __builtin_convertvector(v, bf16x2v);
  return __builtin_bit_cast(unsigned, r);
}
DI float bflo(unsigned u) { return __uint_as_float(u << 16); }
DI float bfhi(unsigned u) { return __uint_as_float(u & 0xffff0000u); }
DI float dot2bf(unsigned a, unsigned b, float c) {
  return __builtin_amdgcn_fdot2_f32_bf16(__builtin_bit_cast(bf16x2v, a), __builtin_bit_cast(bf16x2v, b), c, false);
}

DI void glds16(const void* gsrc, unsigned lds_dst) {
  unsigned keep;
  asm volatile("s_mov_b32 %0, m0\n\ts_mov_b32 m0, %2\n\ts_nop 0\n\tglobal_load_lds_dwordx4 %1, off\n\ts_mov_b32 m0, %0"
               : "=&s"(keep) : "v"(gsrc), "s"(lds_dst) : "memory");
}

enum { MODE_QKV = 0, MODE_RES = 1, MODE_FFN1 = 2 };

template <int BN>
DI unsigned wswz(unsigned row) {
  if constexpr (BN == 128) return ((row & 3) << 2) | ((row >> 2) & 3);
  else return ((row >> 1) & 1) << 2;
}

struct GemmArgs {
  const u16* A;
  const u16* W0;
  const u16* W1;
  const u16* W2;
  const u16* res;
  u16* out0;
  u16* out1;
  u16* out2;
  float* hstat;
  float* csum13;
  float* kmax;
  const u16* w1;
  const u16* w3;
  const u16* out2src;
};

template <int MODE, int BM, int BN, int KTOT, int NST, int DBG = 0>
__global__ __launch_bounds__(256) void gemm_kernel(GemmArgs args) {
  constexpr int WMT = BM / 64, WNT = BN / 64;
  constexpr int XB = BM * 128, WB = 64 * BN * 2, STB = XB + WB;
  constexpr int NXI = BM / 32, NWI = BN / 32, G = NXI + NWI;
  constexpr int NKT = (KTOT + 63) / 64;
  constexpr bool LN = (MODE != MODE_RES);
  constexpr bool LNSTAT = (MODE == MODE_QKV);
  constexpr int LDA = (MODE == MODE_RES) ? KTOT : 512;
  constexpr int LDW = (MODE == MODE_FFN1) ? F_ : 512;
  __shared__ __attribute__((aligned(16))) char sm[NST * STB];
  const int tid = threadIdx.x, lane = tid & 63, wid = tid >> 6;
  const int wm = wid & 1, wn = wid >> 1, h = lane >> 5;
  const int row0 = blockIdx.x * BM, cb = blockIdx.y;
  if constexpr (MODE == MODE_QKV) {
    if (cb >= 9) {
      const int j = blockIdx.x + (cb - 9) * 32;
      if (j < 22) {
        const int cc = tid & 15, rg = tid >> 4;
        const int col = j * 128 + cc * 8;
        float a[8] = {};
        if (col < 2 * F_) {
          const u16* wsrc = (col < F_ ? args.w1 + col : args.w3 + (col - F_)) + (size_t)rg * 32 * F_;
          u32x4 v[32];
#pragma unroll
          for (int r = 0; r < 32; ++r) v[r] = *(const u32x4*)(wsrc + (size_t)r * F_);
#pragma unroll
          for (int r = 0; r < 32; ++r)
#pragma unroll
            for (int e = 0; e < 4; ++e) { a[2 * e] += bflo(v[r][e]); a[2 * e + 1] += bfhi(v[r][e]); }
        }
        float* red = (float*)sm;
#pragma unroll
        for (int e = 0; e < 8; ++e) red[rg * 128 + cc * 8 + e] = a[e];
        __syncthreads();
        if (tid < 128 && j * 128 + tid < 2 * F_) {
          float t = 0.f;
#pragma unroll
          for (int g = 0; g < 16; ++g) t += red[g * 128 + tid];
          args.csum13[j * 128 + tid] = t;
        }
      } else {
        const size_t base = ((size_t)(j - 22) * 16 * 256 + tid) * 16;
        u32x4 t[16];
#pragma unroll
        for (int i = 0; i < 16; ++i) {
          const size_t off = base + (size_t)i * 4096;
          const char* pp = off < 524288 ? (const char*)args.res + off
                         : (off < 524288 + (size_t)F_ * 1024 ? (const char*)args.out2src + (off - 524288) : (const char*)args.res);
          asm volatile("global_load_dwordx4 %0, %1, off" : "=v"(t[i]) : "v"(pp) : "memory");
        }
        asm volatile("s_waitcnt vmcnt(0)" ::: "memory");
#pragma unroll
        for (int i = 0; i < 16; ++i) asm volatile("" ::"v"(t[i]));
      }
      return;
    }
  }

  const u16* xp[NXI];
#pragma unroll
  for (int i = 0; i < NXI; ++i) {
    const int r = (i * 4 + wid) * 8 + (lane >> 3), c = (lane & 7) ^ ((r >> 1) & 7);
    xp[i] = args.A + (size_t)(row0 + r) * LDA + c * 8;
  }
  const u16* wp[NWI];
  int wrow[NWI], wld[NWI];
#pragma unroll
  for (int i = 0; i < NWI; ++i) {
    const int p = (i * 4 + wid) * 64 + lane;
    const int r = p / (BN / 8), c = (p % (BN / 8)) ^ wswz<BN>(r);
    wrow[i] = r;
    if constexpr (MODE == MODE_QKV) {
      if (cb < 8) { wp[i] = args.W0 + cb * 64 + c * 8; wld[i] = 512; }
      else if (c < 2) { wp[i] = args.W1 + c * 8; wld[i] = 16; }
      else { wp[i] = args.W2 + (c & 1) * 8; wld[i] = 16; }
    } else if constexpr (MODE == MODE_RES) {
      wp[i] = args.W0 + cb * BN + c * 8; wld[i] = 512;
    } else {
      const int c32 = c >> 2;
      int f = cb * 64 + (c32 >> 1) * 32 + (c & 3) * 8;
      if (f >= F_) f = 0;
      wp[i] = ((c32 & 1) ? args.W1 : args.W0) + f; wld[i] = F_;
    }
  }
  const unsigned lds0 = (unsigned)(size_t)sm + (unsigned)__builtin_amdgcn_readfirstlane(wid) * 1024u;
  auto issue = [&](int kt, int buf) {
    const unsigned base = lds0 + buf * STB;
#pragma unroll
    for (int i = 0; i < NXI; ++i) glds16(xp[i] + kt * 64, base + i * 4096);
#pragma unroll
    for (int i = 0; i < NWI; ++i) {
      int k = kt * 64 + wrow[i];
      if constexpr (KTOT % 64 != 0) k = k < KTOT ? k : KTOT - 1;
      glds16(wp[i] + (size_t)k * wld[i], base + XB + i * 4096);
    }
  };

  f32x16 acc[WNT][WMT] = {};
  float sx = 0.f, sxx = 0.f, cs = 0.f;
  const int wmu = __builtin_amdgcn_readfirstlane(wm), wnu = __builtin_amdgcn_readfirstlane(wn);
  unsigned xoff[WMT];
#pragma unroll
  for (int t = 0; t < WMT; ++t) xoff[t] = (unsigned)((wm * WMT + t) * 32 + (lane & 31)) * 128u;
  const unsigned xsw = ((lane & 31) >> 1) & 7;
  const unsigned tq = (lane & 15) >> 2, tp = lane & 3, tblk = (lane >> 4) & 1;

  constexpr int PD = NST - 1;
#pragma unroll
  for (int i = 0; i < PD; ++i)
    if (i < NKT) issue(i, i);
  for (int kt = 0; kt < NKT; ++kt) {
    const int rem = NKT - 1 - kt;
    if (rem >= PD - 1) asm volatile("s_waitcnt vmcnt(%0)" ::"n"((PD - 1) * G) : "memory");
    else if (PD >= 3 && rem == 1) asm volatile("s_waitcnt vmcnt(%0)" ::"n"(G) : "memory");
    else if (PD >= 4 && rem == 2) asm volatile("s_waitcnt vmcnt(%0)" ::"n"(2 * G) : "memory");
    else asm volatile("s_waitcnt vmcnt(0)" ::: "memory");
    __builtin_amdgcn_s_barrier();
    asm volatile("" ::: "memory");
    if (kt + PD < NKT && DBG != 2) issue(kt + PD, (kt + PD) % NST);
    const char* Xs = sm + (kt % NST) * STB;
    const char* Ws = Xs + XB;
    const int nks = (KTOT % 64 != 0 && kt == NKT - 1) ? (KTOT % 64) / 16 : 4;
    if (DBG != 1) {
      bf16x8 xb[4][WMT], wa[4][WNT];
#pragma unroll
      for (int ks = 0; ks < 4; ++ks) {
        if (ks < nks) {
#pragma unroll
          for (int t = 0; t < WMT; ++t) xb[ks][t] = *(const bf16x8*)(Xs + xoff[t] + (((2 * ks + h) ^ xsw) << 4));
#pragma unroll
          for (int t = 0; t < WNT; ++t) {
            const unsigned c = (wn * WNT + t) * 4 + 2 * tblk + (tp >> 1);
            const unsigned r0 = 16 * ks + 8 * h + tq, r1 = r0 + 4;
            s16x4 lo = __builtin_amdgcn_ds_read_tr16_b64_v4i16((lds_s16x4*)(Ws + r0 * (BN * 2) + ((c ^ wswz<BN>(r0)) << 4) + 8 * (tp & 1)));
            s16x4 hi = __builtin_amdgcn_ds_read_tr16_b64_v4i16((lds_s16x4*)(Ws + r1 * (BN * 2) + ((c ^ wswz<BN>(r1)) << 4) + 8 * (tp & 1)));
            wa[ks][t] = __builtin_shufflevector(lo, hi, 0, 1, 2, 3, 4, 5, 6, 7);
          }
        }
      }
      __builtin_amdgcn_sched_barrier(0);
#pragma unroll
      for (int ks = 0; ks < 4; ++ks) {
        if (ks < nks) {
#pragma unroll
          for (int tn = 0; tn < WNT; ++tn)
#pragma unroll
            for (int tm = 0; tm < WMT; ++tm) acc[tn][tm] = MFMA32(wa[ks][tn], xb[ks][tm], acc[tn][tm]);
          if constexpr (LNSTAT) {
            static_assert(WMT == 1 && WNT == 1, "in-kernel LN stats assume a 64x64 tile");
            if (wmu == wnu) {
              const u32x4 u = __builtin_bit_cast(u32x4, xb[ks][0]);
#pragma unroll
              for (int j = 0; j < 4; ++j) { sx = dot2bf(u[j], 0x3f803f80u, sx); sxx = dot2bf(u[j], u[j], sxx); }
            } else {
              const u32x4 u = __builtin_bit_cast(u32x4, wa[ks][0]);
#pragma unroll
              for (int j = 0; j < 4; ++j) cs = dot2bf(u[j], 0x3f803f80u, cs);
            }
          }
        }
      }
    }
  }

  float mean[WMT], rstd[WMT];
  if constexpr (LNSTAT) {
    __syncthreads();
    float* csum_s = (float*)sm;
    float* stat_s = (float*)sm + BN;
    if (wmu == wnu) {
      const float s1 = sx + __shfl_xor(sx, 32), s2 = sxx + __shfl_xor(sxx, 32);
      const float mu = s1 * (1.f / 512);
      const float var = fmaxf(s2 * (1.f / 512) - mu * mu, 0.f);
      if (h == 0) *(f32x2*)(stat_s + (wm * 32 + lane) * 2) = (f32x2){mu, 1.f / sqrtf(var + 1e-5f)};
    } else {
      const float c = cs + __shfl_xor(cs, 32);
      if (h == 0) csum_s[wn * 32 + lane] = c;
    }
    __syncthreads();
    const f32x2 st = *(const f32x2*)(stat_s + (wm * 32 + (lane & 31)) * 2);
    mean[0] = st[0]; rstd[0] = st[1];
#pragma unroll
    for (int g = 0; g < 4; ++g) {
      const f32x4 c4 = *(const f32x4*)(csum_s + wn * 32 + 8 * g + 4 * h);
#pragma unroll
      for (int e = 0; e < 4; ++e) acc[0][0][4 * g + e] = rstd[0] * (acc[0][0][4 * g + e] - mean[0] * c4[e]);
    }
  } else if constexpr (LN) {
#pragma unroll
    for (int t = 0; t < WMT; ++t) {
      f32x2 st = {0.f, 0.f};
#pragma unroll
      for (int j = 0; j < 16; ++j) st += *(const f32x2*)(args.hstat + ((size_t)j * S_ + row0 + (wm * WMT + t) * 32 + (lane & 31)) * 2);
      mean[t] = st[0] * (1.f / 512);
      const float var = fmaxf(st[1] * (1.f / 512) - mean[t] * mean[t], 0.f);
      rstd[t] = 1.f / sqrtf(var + 1e-5f);
    }
    const int fb = min(cb * 64 + wn * 32, F_ - 32);
#pragma unroll
    for (int tn = 0; tn < WNT; ++tn) {
#pragma unroll
      for (int g = 0; g < 4; ++g) {
        const f32x4 c4 = *(const f32x4*)(args.csum13 + tn * F_ + fb + 8 * g + 4 * h);
#pragma unroll
        for (int tm = 0; tm < WMT; ++tm)
#pragma unroll
          for (int e = 0; e < 4; ++e) acc[tn][tm][4 * g + e] = rstd[tm] * (acc[tn][tm][4 * g + e] - mean[tm] * c4[e]);
      }
    }
  }

#pragma unroll
  for (int tm = 0; tm < WMT; ++tm) {
    const int m = row0 + (wm * WMT + tm) * 32 + (lane & 31);
    if constexpr (MODE == MODE_QKV) {
      if (cb < 8) {
        const float qs = 0.25f * 1.4426950408889634f;
#pragma unroll
        for (int g = 0; g < 4; ++g) {
          const f32x16& a = acc[0][tm];
          u32x2 o = {pk2(a[4 * g] * qs, a[4 * g + 1] * qs), pk2(a[4 * g + 2] * qs, a[4 * g + 3] * qs)};
          *(u32x2*)(args.out0 + (size_t)m * D_ + cb * 64 + wn * 32 + 8 * g + 4 * h) = o;
        }
      } else if (wn == 0) {
        const f32x16& a = acc[0][tm];
#pragma unroll
        for (int g = 0; g < 2; ++g) {
          u32x2 o = {pk2(a[4 * g], a[4 * g + 1]), pk2(a[4 * g + 2], a[4 * g + 3])};
          *(u32x2*)(args.out1 + (size_t)m * 16 + 8 * g + 4 * h) = o;
        }
        const int kt = m >> 5, ks = (m >> 4) & 1, ka = (m >> 3) & 1, kh = (m >> 2) & 1, kb = m & 3;
        u16* vbase = args.out2 + (size_t)((((kt * 2 + ks) * 2 + kh) * 16) * 8) + 4 * ka + kb;
#pragma unroll
        for (int g = 2; g < 4; ++g)
#pragma unroll
          for (int e = 0; e < 4; ++e) vbase[(8 * (g - 2) + 4 * h + e) * 8] = (u16)(pk2(a[4 * g + e], 0.f) & 0xffffu);
      }
    } else if constexpr (MODE == MODE_RES) {
      float s1 = 0.f, s2 = 0.f;
#pragma unroll
      for (int tn = 0; tn < WNT; ++tn) {
        const size_t base = (size_t)m * D_ + cb * BN + (wn * WNT + tn) * 32 + 4 * h;
#pragma unroll
        for (int g = 0; g < 4; ++g) {
          const f32x16& a = acc[tn][tm];
          const u32x2 rv = *(const u32x2*)(args.res + base + 8 * g);
          u32x2 o = {pk2(a[4 * g] + bflo(rv[0]), a[4 * g + 1] + bfhi(rv[0])),
                     pk2(a[4 * g + 2] + bflo(rv[1]), a[4 * g + 3] + bfhi(rv[1]))};
          *(u32x2*)(args.out0 + base + 8 * g) = o;
          if constexpr (KTOT == 512) {
            s1 = dot2bf(o[0], 0x3f803f80u, s1); s1 = dot2bf(o[1], 0x3f803f80u, s1);
            s2 = dot2bf(o[0], o[0], s2); s2 = dot2bf(o[1], o[1], s2);
          }
        }
      }
      if constexpr (KTOT == 512) {
        s1 += __shfl_xor(s1, 32); s2 += __shfl_xor(s2, 32);
        if (h == 0) *(f32x2*)(args.hstat + ((size_t)(cb * 2 + wn) * S_ + m) * 2) = (f32x2){s1, s2};
      }
    } else {
      const int fb = cb * 64 + wn * 32;
      if (fb < F_) {
        const size_t base = (size_t)m * F_ + fb + 4 * h;
#pragma unroll
        for (int g = 0; g < 4; ++g) {
          float r[4];
#pragma unroll
          for (int e = 0; e < 4; ++e) {
            const float gt = acc[0][tm][4 * g + e], up = acc[1][tm][4 * g + e];
            r[e] = gt * up * __builtin_amdgcn_rcpf(1.f + __builtin_amdgcn_exp2f(gt * -1.4426950408889634f));
          }
          u32x2 o = {pk2(r[0], r[1]), pk2(r[2], r[3])};
          *(u32x2*)(args.out0 + base + 8 * g) = o;
        }
      }
    }
  }
}

template <bool EXACT>
DI void attn_step(f32x16& sc_, f32x16& sn_, f32x16& o, f32x16& negm, bf16x8& kn, const bf16x8 qf,
                  const char* kp, const char* vp0, int vstride, int vd, int t) {
  const bf16x8 v0 = *(const bf16x8*)(vp0 + t * vstride);
  const bf16x8 v1 = *(const bf16x8*)(vp0 + t * vstride + vd);
  sn_ = MFMA32(kn, qf, negm);
  kn = *(const bf16x8*)(kp + ((t + 2) & 31) * 1024);
  if constexpr (EXACT) {
    float m0 = fmaxf(fmaxf(sc_[0], sc_[1]), sc_[2]), m1 = fmaxf(fmaxf(sc_[3], sc_[4]), sc_[5]);
    float m2 = fmaxf(fmaxf(sc_[6], sc_[7]), sc_[8]), m3 = fmaxf(fmaxf(sc_[9], sc_[10]), sc_[11]);
    float m4 = fmaxf(fmaxf(sc_[12], sc_[13]), sc_[14]);
    float mx = fmaxf(fmaxf(fmaxf(m0, m1), sc_[15]), fmaxf(fmaxf(m2, m3), m4));
    if (__builtin_expect(__any(mx > 8.f), 0)) {
      mx = fmaxf(mx, __shfl_xor(mx, 32));
      mx = fmaxf(mx, 0.f);
      const float sc = __builtin_amdgcn_exp2f(-mx);
      o *= sc; negm -= mx; sc_ -= mx; sn_ -= mx;
    }
  }
  u32x4 p0, p1;
#pragma unroll
  for (int i = 0; i < 4; ++i) {
    p0[i] = pk2(__builtin_amdgcn_exp2f(sc_[2 * i]), __builtin_amdgcn_exp2f(sc_[2 * i + 1]));
    p1[i] = pk2(__builtin_amdgcn_exp2f(sc_[8 + 2 * i]), __builtin_amdgcn_exp2f(sc_[9 + 2 * i]));
  }
  o = MFMA32(v0, __builtin_bit_cast(bf16x8, p0), o);
  o = MFMA32(v1, __builtin_bit_cast(bf16x8, p1), o);
}

constexpr int ATT_LDS = 131072 + 64;
__global__ __launch_bounds__(1024) void attn_kernel(const u16* __restrict__ Q, const u16* __restrict__ Kf,
                                                    const u16* __restrict__ Vf, u16* __restrict__ O) {
  __shared__ __attribute__((aligned(16))) char sm[ATT_LDS];
  const int tid = threadIdx.x, lane = tid & 63, wid = tid >> 6;
  const int widu = __builtin_amdgcn_readfirstlane(wid);
  const int h = lane >> 5, r = lane & 31;
  const int q = blockIdx.x * 8 + (wid & 7), half = wid >> 3;
  bf16x8 qf;
  asm volatile("global_load_dwordx4 %0, %1, off" : "=v"(qf) : "v"(Q + (size_t)q * D_ + r * 16 + h * 8) : "memory");
  {
    const unsigned lds0 = (unsigned)(size_t)sm;
    const int hf = (widu >> 2) & 1, isv = widu >> 3, pc = widu & 3;
    const unsigned off0 = isv * 65536 + hf * 32768 + pc * 1024;
    const char* src = (const char*)(isv ? Vf : Kf) + hf * 32768 + pc * 1024 + lane * 16;
#pragma unroll
    for (int c = 0; c < 8; ++c) glds16(src + c * 4096, lds0 + off0 + c * 4096);
    if (tid < 8) ((unsigned*)(sm + 131072))[tid] = tid < 4 ? 0x3f803f80u : 0u;
  }
  asm volatile("s_waitcnt vmcnt(0) lgkmcnt(0)" : "+v"(qf)::"memory");
  __builtin_amdgcn_s_barrier();
  asm volatile("" ::: "memory");
  const char* kp = sm + half * 32768 + r * 32 + h * 16;
  const char* vp0 = sm + (r < 16 ? 65536 + half * 32768 + h * 256 + r * 16 : (r == 16 ? 131072 : 131088));
  const int vd = r < 16 ? 512 : 0;
  const int vstride = r < 16 ? 1024 : 0;
  f32x16 negm, sA, sB = {};
  {
    f32x16 z = {};
    f32x16 s0 = MFMA32(*(const bf16x8*)kp, qf, z);
    float mx = s0[0];
#pragma unroll
    for (int i = 1; i < 16; ++i) mx = fmaxf(mx, s0[i]);
    mx = fmaxf(mx, __shfl_xor(mx, 32));
    negm = (f32x16)(-mx);
    sA = s0 + negm;
  }
  f32x16 o = {};
  bf16x8 kn = *(const bf16x8*)(kp + 1024);
  for (int t = 0; t < 32; t += 2) {
    attn_step<false>(sA, sB, o, negm, kn, qf, kp, vp0, vstride, vd, t);
    attn_step<false>(sB, sA, o, negm, kn, qf, kp, vp0, vstride, vd, t + 1);
  }
  {
    const float l = __shfl(o[8], r);
    if (__any(!(l < 1e30f))) {
      o = (f32x16){};
      sA = MFMA32(*(const bf16x8*)kp, qf, negm);
      kn = *(const bf16x8*)(kp + 1024);
      for (int t = 0; t < 32; t += 2) {
        attn_step<true>(sA, sB, o, negm, kn, qf, kp, vp0, vstride, vd, t);
        attn_step<true>(sB, sA, o, negm, kn, qf, kp, vp0, vstride, vd, t + 1);
      }
    }
  }
  __syncthreads();
  float* ex = (float*)sm + (wid & 7) * (64 * 10);
  if (half == 1) {
#pragma unroll
    for (int i = 0; i < 9; ++i) ex[i * 64 + lane] = o[i];
    ex[9 * 64 + lane] = negm[0];
  }
  __syncthreads();
  if (half == 0) {
    const float nm1 = ex[9 * 64 + lane], nm0 = negm[0];
    const float nm = fminf(nm0, nm1);
    const float a0 = __builtin_amdgcn_exp2f(nm - nm0), a1 = __builtin_amdgcn_exp2f(nm - nm1);
#pragma unroll
    for (int i = 0; i < 9; ++i) o[i] = o[i] * a0 + ex[i * 64 + lane] * a1;
    const float l = __shfl(o[8], r);
    const float rl = 1.f / l;
    u32x2 oa = {pk2(o[0] * rl, o[1] * rl), pk2(o[2] * rl, o[3] * rl)};
    u32x2 ob = {pk2(o[4] * rl, o[5] * rl), pk2(o[6] * rl, o[7] * rl)};
    *(u32x2*)(O + (size_t)q * D_ + r * 16 + 4 * h) = oa;
    *(u32x2*)(O + (size_t)q * D_ + r * 16 + 8 + 4 * h) = ob;
  }
}

extern "C" void kernel_launch(void* const* d_in, const int* in_sizes, int n_in, void* d_out, int out_size,
                              void* d_ws, size_t ws_size, hipStream_t stream) {
  const u16* x = (const u16*)d_in[0];
  const u16* wq = (const u16*)d_in[1];
  const u16* wk = (const u16*)d_in[2];
  const u16* wv = (const u16*)d_in[3];
  const u16* wo = (const u16*)d_in[4];
  const u16* w1 = (const u16*)d_in[6];
  const u16* w3 = (const u16*)d_in[7];
  const u16* w2 = (const u16*)d_in[8];
  u16* out = (u16*)d_out;
  char* ws = (char*)d_ws;
  u16* Q = (u16*)(ws);
  u16* Kf = (u16*)(ws + (2u << 20));
  u16* Vf = (u16*)(ws + (2u << 20) + 65536);
  u16* O = (u16*)(ws + (3u << 20));
  u16* Hb = (u16*)(ws + (5u << 20));
  u16* ACT = (u16*)(ws + (7u << 20));
  float* hstat = (float*)(ws + (2u << 20) + 131072);
  float* csum13 = hstat + 16 * 4096;
  {
    GemmArgs a = {x, wq, wk, wv, wo, Q, Kf, Vf, hstat, csum13, nullptr, w1, w3, w2};
    gemm_kernel<MODE_QKV, 64, 64, 512, 4><<<dim3(32, 11), 256, 0, stream>>>(a);
  }
  attn_kernel<<<dim3(256), 1024, 0, stream>>>(Q, Kf, Vf, O);
  {
    GemmArgs a = {O, wo, nullptr, nullptr, x, Hb, nullptr, nullptr, hstat, nullptr, nullptr, nullptr, nullptr, nullptr};
    gemm_kernel<MODE_RES, 64, 64, 512, 4><<<dim3(32, 8), 256, 0, stream>>>(a);
  }
  {
    GemmArgs a = {Hb, w1, w3, nullptr, nullptr, ACT, nullptr, nullptr, hstat, csum13, nullptr, nullptr, nullptr, nullptr};
    gemm_kernel<MODE_FFN1, 128, 128, 512, 2><<<dim3(16, 22), 256, 0, stream>>>(a);
  }
  {
    GemmArgs a = {ACT, w2, nullptr, nullptr, Hb, out, nullptr, nullptr, nullptr, nullptr, nullptr, nullptr, nullptr, nullptr};
    gemm_kernel<MODE_RES, 64, 64, 1376, 4><<<dim3(32, 8), 256, 0, stream>>>(a);
  }
}
```

```cpp
#include <hip/hip_runtime.h>
#include <hip/hip_bf16.h>
#include <stdint.h>
#include <type_traits>


typedef short bf16x8 __attribute__((ext_vector_type(8)));
typedef short s16x4 __attribute__((ext_vector_type(4)));
typedef float f32x16 __attribute__((ext_vector_type(16)));
typedef float f32x4 __attribute__((ext_vector_type(4)));
typedef float f32x2 __attribute__((ext_vector_type(2)));
typedef __bf16 bf16x2v __attribute__((ext_vector_type(2)));
typedef unsigned u32x4 __attribute__((ext_vector_type(4)));
typedef unsigned u32x2 __attribute__((ext_vector_type(2)));
typedef __attribute__((address_space(3))) s16x4 lds_s16x4;
typedef unsigned short u16;

#define DI __device__ __forceinline__
#define MFMA32(a, b, c) __builtin_amdgcn_mfma_f32_32x32x16_bf16((a), (b), (c), 0, 0, 0)

constexpr int S_ = 2048, D_ = 512, F_ = 1376;

DI unsigned pk2(float a, float b) {
  f32x2 v = {a, b};
  bf16x2v r = __builtin_convertvector(v, bf16x2v);
  return __builtin_bit_cast(unsigned, r);
}
DI float bflo(unsigned u) { return __uint_as_float(u << 16); }
DI float bfhi(unsigned u) { return __uint_as_float(u & 0xffff0000u); }
DI float dot2bf(unsigned a, unsigned b, float c) {
  return __builtin_amdgcn_fdot2_f32_bf16(__builtin_bit_cast(bf16x2v, a), __builtin_bit_cast(bf16x2v, b), c, false);
}

DI void glds16(const void* gsrc, unsigned lds_dst) {
  unsigned keep;
  asm volatile("s_mov_b32 %0, m0\n\ts_mov_b32 m0, %2\n\ts_nop 0\n\tglobal_load_lds_dwordx4 %1, off\n\ts_mov_b32 m0, %0"
               : "=&s"(keep) : "v"(gsrc), "s"(lds_dst) : "memory");
}

enum { MODE_QKV = 0, MODE_RES = 1, MODE_FFN1 = 2 };

template <int BN>
DI unsigned wswz(unsigned row) {
  if constexpr (BN == 128) return ((row & 3) << 2) | ((row >> 2) & 3);
  else return ((row >> 1) & 1) << 2;
}

struct GemmArgs {
  const u16* A;
  const u16* W0;
  const u16* W1;
  const u16* W2;
  const u16* res;
  u16* out0;
  u16* out1;
  u16* out2;
  float* hstat;
  float* csum13;
  float* kmax;
  const u16* w1;
  const u16* w3;
  const u16* out2src;
};

template <int MODE, int BM, int BN, int KTOT, int NST, int DBG = 0, int WGM = 2>
__global__ __launch_bounds__(256) void gemm_kernel(GemmArgs args) {
  constexpr int WGN = 4 / WGM, WMT = BM / (32 * WGM), WNT = BN / (32 * WGN);
  constexpr int XB = BM * 128, WB = 64 * BN * 2, STB = XB + WB;
  constexpr int NXI = BM / 32, NWI = BN / 32, G = NXI + NWI;
  constexpr int NKT = (KTOT + 63) / 64;
  constexpr bool LN = (MODE != MODE_RES);
  constexpr bool LNSTAT = (MODE == MODE_QKV);
  constexpr int LDA = (MODE == MODE_RES) ? KTOT : 512;
  constexpr int LDW = (MODE == MODE_FFN1) ? F_ : 512;
  __shared__ __attribute__((aligned(16))) char sm[NST * STB];
  const int tid = threadIdx.x, lane = tid & 63, wid = tid >> 6;
  const int wm = wid % WGM, wn = wid / WGM, h = lane >> 5;
  const int row0 = blockIdx.x * BM, cb = blockIdx.y;
  if constexpr (MODE == MODE_QKV) {
    if (cb >= 9) {
      const int j = blockIdx.x + (cb - 9) * 32;
      if (j < 22) {
        const int cc = tid & 15, rg = tid >> 4;
        const int col = j * 128 + cc * 8;
        float a[8] = {};
        if (col < 2 * F_) {
          const u16* wsrc = (col < F_ ? args.w1 + col : args.w3 + (col - F_)) + (size_t)rg * 32 * F_;
          u32x4 v[32];
#pragma unroll
          for (int r = 0; r < 32; ++r) v[r] = *(const u32x4*)(wsrc + (size_t)r * F_);
#pragma unroll
          for (int r = 0; r < 32; ++r)
#pragma unroll
            for (int e = 0; e < 4; ++e) { a[2 * e] += bflo(v[r][e]); a[2 * e + 1] += bfhi(v[r][e]); }
        }
        float* red = (float*)sm;
#pragma unroll
        for (int e = 0; e < 8; ++e) red[rg * 128 + cc * 8 + e] = a[e];
        __syncthreads();
        if (tid < 128 && j * 128 + tid < 2 * F_) {
          float t = 0.f;
#pragma unroll
          for (int g = 0; g < 16; ++g) t += red[g * 128 + tid];
          args.csum13[j * 128 + tid] = t;
        }
      } else {
        const size_t base = ((size_t)(j - 22) * 16 * 256 + tid) * 16;
        u32x4 t[16];
#pragma unroll
        for (int i = 0; i < 16; ++i) {
          const size_t off = base + (size_t)i * 4096;
          const char* pp = off < 524288 ? (const char*)args.res + off
                         : (off < 524288 + (size_t)F_ * 1024 ? (const char*)args.out2src + (off - 524288) : (const char*)args.res);
          asm volatile("global_load_dwordx4 %0, %1, off" : "=v"(t[i]) : "v"(pp) : "memory");
        }
        asm volatile("s_waitcnt vmcnt(0)" ::: "memory");
#pragma unroll
        for (int i = 0; i < 16; ++i) asm volatile("" ::"v"(t[i]));
      }
      return;
    }
  }

  const u16* xp[NXI];
#pragma unroll
  for (int i = 0; i < NXI; ++i) {
    const int r = (i * 4 + wid) * 8 + (lane >> 3), c = (lane & 7) ^ ((r >> 1) & 7);
    xp[i] = args.A + (size_t)(row0 + r) * LDA + c * 8;
  }
  const u16* wp[NWI];
  int wrow[NWI], wld[NWI];
#pragma unroll
  for (int i = 0; i < NWI; ++i) {
    const int p = (i * 4 + wid) * 64 + lane;
    const int r = p / (BN / 8), c = (p % (BN / 8)) ^ wswz<BN>(r);
    wrow[i] = r;
    if constexpr (MODE == MODE_QKV) {
      if (cb < 8) { wp[i] = args.W0 + cb * 64 + c * 8; wld[i] = 512; }
      else if (c < 2) { wp[i] = args.W1 + c * 8; wld[i] = 16; }
      else { wp[i] = args.W2 + (c & 1) * 8; wld[i] = 16; }
    } else if constexpr (MODE == MODE_RES) {
      wp[i] = args.W0 + cb * BN + c * 8; wld[i] = 512;
    } else {
      const int c32 = c >> 2;
      int f = cb * (BN / 2) + (c32 >> 1) * 32 + (c & 3) * 8;
      if (f >= F_) f = 0;
      wp[i] = ((c32 & 1) ? args.W1 : args.W0) + f; wld[i] = F_;
    }
  }
  const unsigned lds0 = (unsigned)(size_t)sm + (unsigned)__builtin_amdgcn_readfirstlane(wid) * 1024u;
  auto issue = [&](int kt, int buf) {
    const unsigned base = lds0 + buf * STB;
#pragma unroll
    for (int i = 0; i < NXI; ++i) glds16(xp[i] + kt * 64, base + i * 4096);
#pragma unroll
    for (int i = 0; i < NWI; ++i) {
      int k = kt * 64 + wrow[i];
      if constexpr (KTOT % 64 != 0) k = k < KTOT ? k : KTOT - 1;
      glds16(wp[i] + (size_t)k * wld[i], base + XB + i * 4096);
    }
  };

  f32x16 acc[WNT][WMT] = {};
  float sx = 0.f, sxx = 0.f, cs = 0.f;
  const int wmu = __builtin_amdgcn_readfirstlane(wm), wnu = __builtin_amdgcn_readfirstlane(wn);
  unsigned xoff[WMT];
#pragma unroll
  for (int t = 0; t < WMT; ++t) xoff[t] = (unsigned)((wm * WMT + t) * 32 + (lane & 31)) * 128u;
  const unsigned xsw = ((lane & 31) >> 1) & 7;
  const unsigned tq = (lane & 15) >> 2, tp = lane & 3, tblk = (lane >> 4) & 1;

  constexpr int PD = NST - 1;
#pragma unroll
  for (int i = 0; i < PD; ++i)
    if (i < NKT) issue(i, i);
  for (int kt = 0; kt < NKT; ++kt) {
    const int rem = NKT - 1 - kt;
    if (rem >= PD - 1) asm volatile("s_waitcnt vmcnt(%0)" ::"n"((PD - 1) * G) : "memory");
    else if (PD >= 3 && rem == 1) asm volatile("s_waitcnt vmcnt(%0)" ::"n"(G) : "memory");
    else if (PD >= 4 && rem == 2) asm volatile("s_waitcnt vmcnt(%0)" ::"n"(2 * G) : "memory");
    else asm volatile("s_waitcnt vmcnt(0)" ::: "memory");
    __builtin_amdgcn_s_barrier();
    asm volatile("" ::: "memory");
    if (kt + PD < NKT && DBG != 2) issue(kt + PD, (kt + PD) % NST);
    const char* Xs = sm + (kt % NST) * STB;
    const char* Ws = Xs + XB;
    const int nks = (KTOT % 64 != 0 && kt == NKT - 1) ? (KTOT % 64) / 16 : 4;
    if (DBG != 1) {
      bf16x8 xb[4][WMT], wa[4][WNT];
#pragma unroll
      for (int ks = 0; ks < 4; ++ks) {
        if (ks < nks) {
#pragma unroll
          for (int t = 0; t < WMT; ++t) xb[ks][t] = *(const bf16x8*)(Xs + xoff[t] + (((2 * ks + h) ^ xsw) << 4));
#pragma unroll
          for (int t = 0; t < WNT; ++t) {
            const unsigned c = (wn * WNT + t) * 4 + 2 * tblk + (tp >> 1);
            const unsigned r0 = 16 * ks + 8 * h + tq, r1 = r0 + 4;
            s16x4 lo = __builtin_amdgcn_ds_read_tr16_b64_v4i16((lds_s16x4*)(Ws + r0 * (BN * 2) + ((c ^ wswz<BN>(r0)) << 4) + 8 * (tp & 1)));
            s16x4 hi = __builtin_amdgcn_ds_read_tr16_b64_v4i16((lds_s16x4*)(Ws + r1 * (BN * 2) + ((c ^ wswz<BN>(r1)) << 4) + 8 * (tp & 1)));
            wa[ks][t] = __builtin_shufflevector(lo, hi, 0, 1, 2, 3, 4, 5, 6, 7);
          }
        }
      }
      __builtin_amdgcn_sched_barrier(0);
#pragma unroll
      for (int ks = 0; ks < 4; ++ks) {
        if (ks < nks) {
#pragma unroll
          for (int tn = 0; tn < WNT; ++tn)
#pragma unroll
            for (int tm = 0; tm < WMT; ++tm) acc[tn][tm] = MFMA32(wa[ks][tn], xb[ks][tm], acc[tn][tm]);
          if constexpr (LNSTAT) {
            static_assert(WMT == 1 && WNT == 1, "in-kernel LN stats assume a 64x64 tile");
            if (wmu == wnu) {
              const u32x4 u = __builtin_bit_cast(u32x4, xb[ks][0]);
#pragma unroll
              for (int j = 0; j < 4; ++j) { sx = dot2bf(u[j], 0x3f803f80u, sx); sxx = dot2bf(u[j], u[j], sxx); }
            } else {
              const u32x4 u = __builtin_bit_cast(u32x4, wa[ks][0]);
#pragma unroll
              for (int j = 0; j < 4; ++j) cs = dot2bf(u[j], 0x3f803f80u, cs);
            }
          }
        }
      }
    }
  }

  float mean[WMT], rstd[WMT];
  if constexpr (LNSTAT) {
    __syncthreads();
    float* csum_s = (float*)sm;
    float* stat_s = (float*)sm + BN;
    if (wmu == wnu) {
      const float s1 = sx + __shfl_xor(sx, 32), s2 = sxx + __shfl_xor(sxx, 32);
      const float mu = s1 * (1.f / 512);
      const float var = fmaxf(s2 * (1.f / 512) - mu * mu, 0.f);
      if (h == 0) *(f32x2*)(stat_s + (wm * 32 + lane) * 2) = (f32x2){mu, 1.f / sqrtf(var + 1e-5f)};
    } else {
      const float c = cs + __shfl_xor(cs, 32);
      if (h == 0) csum_s[wn * 32 + lane] = c;
    }
    __syncthreads();
    const f32x2 st = *(const f32x2*)(stat_s + (wm * 32 + (lane & 31)) * 2);
    mean[0] = st[0]; rstd[0] = st[1];
#pragma unroll
    for (int g = 0; g < 4; ++g) {
      const f32x4 c4 = *(const f32x4*)(csum_s + wn * 32 + 8 * g + 4 * h);
#pragma unroll
      for (int e = 0; e < 4; ++e) acc[0][0][4 * g + e] = rstd[0] * (acc[0][0][4 * g + e] - mean[0] * c4[e]);
    }
  } else if constexpr (LN) {
#pragma unroll
    for (int t = 0; t < WMT; ++t) {
      f32x2 st = {0.f, 0.f};
#pragma unroll
      for (int j = 0; j < 8; ++j) st += *(const f32x2*)(args.hstat + ((size_t)j * S_ + row0 + (wm * WMT + t) * 32 + (lane & 31)) * 2);
      mean[t] = st[0] * (1.f / 512);
      const float var = fmaxf(st[1] * (1.f / 512) - mean[t] * mean[t], 0.f);
      rstd[t] = 1.f / sqrtf(var + 1e-5f);
    }
    const int fb = min(cb * (BN / 2) + wn * 32, F_ - 32);
#pragma unroll
    for (int tn = 0; tn < WNT; ++tn) {
#pragma unroll
      for (int g = 0; g < 4; ++g) {
        const f32x4 c4 = *(const f32x4*)(args.csum13 + tn * F_ + fb + 8 * g + 4 * h);
#pragma unroll
        for (int tm = 0; tm < WMT; ++tm)
#pragma unroll
          for (int e = 0; e < 4; ++e) acc[tn][tm][4 * g + e] = rstd[tm] * (acc[tn][tm][4 * g + e] - mean[tm] * c4[e]);
      }
    }
  }

  if constexpr (MODE == MODE_QKV) {
    if (cb == 8) {
      if (wn == 0) {
        const int m = row0 + wm * 32 + (lane & 31);
        const f32x16& a = acc[0][0];
#pragma unroll
        for (int g = 0; g < 2; ++g) {
          u32x2 o = {pk2(a[4 * g], a[4 * g + 1]), pk2(a[4 * g + 2], a[4 * g + 3])};
          *(u32x2*)(args.out1 + (size_t)m * 16 + 8 * g + 4 * h) = o;
        }
        const int kt = m >> 5, ks = (m >> 4) & 1, ka = (m >> 3) & 1, kh = (m >> 2) & 1, kb = m & 3;
        u16* vbase = args.out2 + (size_t)((((kt * 2 + ks) * 2 + kh) * 16) * 8) + 4 * ka + kb;
#pragma unroll
        for (int g = 2; g < 4; ++g)
#pragma unroll
          for (int e = 0; e < 4; ++e) vbase[(8 * (g - 2) + 4 * h + e) * 8] = (u16)(pk2(a[4 * g + e], 0.f) & 0xffffu);
      }
      return;
    }
  }
  constexpr int NC = (MODE == MODE_FFN1) ? BN / 2 : BN;
  constexpr int SSTR = NC + 4;
  static_assert(BM * SSTR * 4 <= NST * STB, "staging tile must fit");
  float* stg = (float*)sm;
  __syncthreads();
#pragma unroll
  for (int tm = 0; tm < WMT; ++tm) {
    float* srow = stg + ((wm * WMT + tm) * 32 + (lane & 31)) * SSTR + 4 * h;
    if constexpr (MODE == MODE_FFN1) {
#pragma unroll
      for (int g = 0; g < 4; ++g) {
        f32x4 r;
#pragma unroll
        for (int e = 0; e < 4; ++e) {
          const unsigned gu = pk2(acc[0][tm][4 * g + e], acc[1][tm][4 * g + e]);
          const float gt = bflo(gu), up = bfhi(gu);
          const float sg = gt * __builtin_amdgcn_rcpf(1.f + __builtin_amdgcn_exp2f(gt * -1.4426950408889634f));
          r[e] = bflo(pk2(sg, 0.f)) * up;
        }
        *(f32x4*)(srow + wn * 32 + 8 * g) = r;
      }
    } else {
#pragma unroll
      for (int tn = 0; tn < WNT; ++tn)
#pragma unroll
        for (int g = 0; g < 4; ++g) {
          const f32x16& a = acc[tn][tm];
          *(f32x4*)(srow + (wn * WNT + tn) * 32 + 8 * g) = (f32x4){a[4 * g], a[4 * g + 1], a[4 * g + 2], a[4 * g + 3]};
        }
    }
  }
  __syncthreads();
  constexpr int CPR = NC / 8;
  constexpr int RPP = 256 / CPR;
  const int cc = tid % CPR, rr = tid / CPR;
#pragma unroll
  for (int pss = 0; pss < BM / RPP; ++pss) {
    const int rl = pss * RPP + rr, m = row0 + rl;
    f32x4 v0 = *(const f32x4*)(stg + rl * SSTR + cc * 8), v1 = *(const f32x4*)(stg + rl * SSTR + cc * 8 + 4);
    if constexpr (MODE == MODE_QKV) {
      const float qs = 0.25f * 1.4426950408889634f;
      v0 *= qs; v1 *= qs;
      u32x4 o = {pk2(v0[0], v0[1]), pk2(v0[2], v0[3]), pk2(v1[0], v1[1]), pk2(v1[2], v1[3])};
      *(u32x4*)(args.out0 + (size_t)m * D_ + cb * BN + cc * 8) = o;
    } else if constexpr (MODE == MODE_RES) {
      const size_t off = (size_t)m * D_ + cb * BN + cc * 8;
      const u32x4 rv = *(const u32x4*)(args.res + off);
      u32x4 o = {pk2(v0[0] + bflo(rv[0]), v0[1] + bfhi(rv[0])), pk2(v0[2] + bflo(rv[1]), v0[3] + bfhi(rv[1])),
                 pk2(v1[0] + bflo(rv[2]), v1[1] + bfhi(rv[2])), pk2(v1[2] + bflo(rv[3]), v1[3] + bfhi(rv[3]))};
      *(u32x4*)(args.out0 + off) = o;
      if constexpr (KTOT == 512) {
        float s1 = 0.f, s2 = 0.f;
#pragma unroll
        for (int j = 0; j < 4; ++j) { s1 = dot2bf(o[j], 0x3f803f80u, s1); s2 = dot2bf(o[j], o[j], s2); }
#pragma unroll
        for (int d = 1; d < CPR; d <<= 1) { s1 += __shfl_xor(s1, d); s2 += __shfl_xor(s2, d); }
        if (cc == 0) *(f32x2*)(args.hstat + ((size_t)cb * S_ + m) * 2) = (f32x2){s1, s2};
      }
    } else {
      const int f = cb * NC + cc * 8;
      if (f < F_) {
        u32x4 o = {pk2(v0[0], v0[1]), pk2(v0[2], v0[3]), pk2(v1[0], v1[1]), pk2(v1[2], v1[3])};
        *(u32x4*)(args.out0 + (size_t)m * F_ + f) = o;
      }
    }
  }
}

template <bool EXACT>
DI void attn_step(f32x16& sc_, f32x16& sn_, f32x16& o, f32x16& negm, bf16x8& kn, const bf16x8 qf,
                  const char* kp, const char* vp0, int vstride, int vd, int t) {
  const bf16x8 v0 = *(const bf16x8*)(vp0 + t * vstride);
  const bf16x8 v1 = *(const bf16x8*)(vp0 + t * vstride + vd);
  sn_ = MFMA32(kn, qf, negm);
  kn = *(const bf16x8*)(kp + ((t + 2) & 31) * 1024);
  if constexpr (EXACT) {
    float m0 = fmaxf(fmaxf(sc_[0], sc_[1]), sc_[2]), m1 = fmaxf(fmaxf(sc_[3], sc_[4]), sc_[5]);
    float m2 = fmaxf(fmaxf(sc_[6], sc_[7]), sc_[8]), m3 = fmaxf(fmaxf(sc_[9], sc_[10]), sc_[11]);
    float m4 = fmaxf(fmaxf(sc_[12], sc_[13]), sc_[14]);
    float mx = fmaxf(fmaxf(fmaxf(m0, m1), sc_[15]), fmaxf(fmaxf(m2, m3), m4));
    if (__builtin_expect(__any(mx > 8.f), 0)) {
      mx = fmaxf(mx, __shfl_xor(mx, 32));
      mx = fmaxf(mx, 0.f);
      const float sc = __builtin_amdgcn_exp2f(-mx);
      o *= sc; negm -= mx; sc_ -= mx; sn_ -= mx;
    }
  }
  u32x4 p0, p1;
#pragma unroll
  for (int i = 0; i < 4; ++i) {
    p0[i] = pk2(__builtin_amdgcn_exp2f(sc_[2 * i]), __builtin_amdgcn_exp2f(sc_[2 * i + 1]));
    p1[i] = pk2(__builtin_amdgcn_exp2f(sc_[8 + 2 * i]), __builtin_amdgcn_exp2f(sc_[9 + 2 * i]));
  }
  o = MFMA32(v0, __builtin_bit_cast(bf16x8, p0), o);
  o = MFMA32(v1, __builtin_bit_cast(bf16x8, p1), o);
}

constexpr int ATT_LDS = 131072 + 64;
__global__ __launch_bounds__(1024) void attn_kernel(const u16* __restrict__ Q, const u16* __restrict__ Kf,
                                                    const u16* __restrict__ Vf, u16* __restrict__ O) {
  __shared__ __attribute__((aligned(16))) char sm[ATT_LDS];
  const int tid = threadIdx.x, lane = tid & 63, wid = tid >> 6;
  const int widu = __builtin_amdgcn_readfirstlane(wid);
  const int h = lane >> 5, r = lane & 31;
  const int q = blockIdx.x * 8 + (wid & 7), half = wid >> 3;
  bf16x8 qf;
  asm volatile("global_load_dwordx4 %0, %1, off" : "=v"(qf) : "v"(Q + (size_t)q * D_ + r * 16 + h * 8) : "memory");
  {
    const unsigned lds0 = (unsigned)(size_t)sm;
    const int hf = (widu >> 2) & 1, isv = widu >> 3, pc = widu & 3;
    const unsigned off0 = isv * 65536 + hf * 32768 + pc * 2048;
    const char* src = (const char*)(isv ? Vf : Kf) + hf * 32768 + pc * 2048 + lane * 16;
#pragma unroll
    for (int c = 0; c < 4; ++c) {
      glds16(src + c * 8192, lds0 + off0 + c * 8192);
      glds16(src + c * 8192 + 1024, lds0 + off0 + c * 8192 + 1024);
    }
    if (tid < 8) ((unsigned*)(sm + 131072))[tid] = tid < 4 ? 0x3f803f80u : 0u;
  }
  asm volatile("s_waitcnt vmcnt(6) lgkmcnt(0)" : "+v"(qf)::"memory");
  __builtin_amdgcn_s_barrier();
  asm volatile("" ::: "memory");
  const char* kp = sm + half * 32768 + r * 32 + h * 16;
  const char* vp0 = sm + (r < 16 ? 65536 + half * 32768 + h * 256 + r * 16 : (r == 16 ? 131072 : 131088));
  const int vd = r < 16 ? 512 : 0;
  const int vstride = r < 16 ? 1024 : 0;
  f32x16 negm, sA, sB = {};
  {
    f32x16 z = {};
    f32x16 s0 = MFMA32(*(const bf16x8*)kp, qf, z);
    float mx = s0[0];
#pragma unroll
    for (int i = 1; i < 16; ++i) mx = fmaxf(mx, s0[i]);
    mx = fmaxf(mx, __shfl_xor(mx, 32));
    negm = (f32x16)(-mx);
    sA = s0 + negm;
  }
  f32x16 o = {};
  bf16x8 kn = *(const bf16x8*)(kp + 1024);
#define ATT_SEG(T0, T1)                                                            \
  for (int t = T0; t < T1; t += 2) {                                               \
    attn_step<false>(sA, sB, o, negm, kn, qf, kp, vp0, vstride, vd, t);            \
    attn_step<false>(sB, sA, o, negm, kn, qf, kp, vp0, vstride, vd, t + 1);        \
  }
#define ATT_SYNC(N)                                                                \
  asm volatile("s_waitcnt vmcnt(" #N ")" ::: "memory");                            \
  __builtin_amdgcn_s_barrier();                                                    \
  asm volatile("" ::: "memory");
  ATT_SEG(0, 6)
  ATT_SYNC(4)
  ATT_SEG(6, 14)
  ATT_SYNC(2)
  ATT_SEG(14, 22)
  ATT_SYNC(0)
  ATT_SEG(22, 32)
  {
    const float l = __shfl(o[8], r);
    if (__any(!(l < 1e30f))) {
      o = (f32x16){};
      sA = MFMA32(*(const bf16x8*)kp, qf, negm);
      kn = *(const bf16x8*)(kp + 1024);
      for (int t = 0; t < 32; t += 2) {
        attn_step<true>(sA, sB, o, negm, kn, qf, kp, vp0, vstride, vd, t);
        attn_step<true>(sB, sA, o, negm, kn, qf, kp, vp0, vstride, vd, t + 1);
      }
    }
  }
  __syncthreads();
  float* ex = (float*)sm + (wid & 7) * (64 * 10);
  if (half == 1) {
#pragma unroll
    for (int i = 0; i < 9; ++i) ex[i * 64 + lane] = o[i];
    ex[9 * 64 + lane] = negm[0];
  }
  __syncthreads();
  if (half == 0) {
    const float nm1 = ex[9 * 64 + lane], nm0 = negm[0];
    const float nm = fminf(nm0, nm1);
    const float a0 = __builtin_amdgcn_exp2f(nm - nm0), a1 = __builtin_amdgcn_exp2f(nm - nm1);
#pragma unroll
    for (int i = 0; i < 9; ++i) o[i] = o[i] * a0 + ex[i * 64 + lane] * a1;
    const float l = __shfl(o[8], r);
    const float rl = 1.f / l;
    u32x2 oa = {pk2(o[0] * rl, o[1] * rl), pk2(o[2] * rl, o[3] * rl)};
    u32x2 ob = {pk2(o[4] * rl, o[5] * rl), pk2(o[6] * rl, o[7] * rl)};
    *(u32x2*)(O + (size_t)q * D_ + r * 16 + 4 * h) = oa;
    *(u32x2*)(O + (size_t)q * D_ + r * 16 + 8 + 4 * h) = ob;
  }
}

extern "C" void kernel_launch(void* const* d_in, const int* in_sizes, int n_in, void* d_out, int out_size,
                              void* d_ws, size_t ws_size, hipStream_t stream) {
  const u16* x = (const u16*)d_in[0];
  const u16* wq = (const u16*)d_in[1];
  const u16* wk = (const u16*)d_in[2];
  const u16* wv = (const u16*)d_in[3];
  const u16* wo = (const u16*)d_in[4];
  const u16* w1 = (const u16*)d_in[6];
  const u16* w3 = (const u16*)d_in[7];
  const u16* w2 = (const u16*)d_in[8];
  u16* out = (u16*)d_out;
  char* ws = (char*)d_ws;
  u16* Q = (u16*)(ws);
  u16* Kf = (u16*)(ws + (2u << 20));
  u16* Vf = (u16*)(ws + (2u << 20) + 65536);
  u16* O = (u16*)(ws + (3u << 20));
  u16* Hb = (u16*)(ws + (5u << 20));
  u16* ACT = (u16*)(ws + (7u << 20));
  float* hstat = (float*)(ws + (2u << 20) + 131072);
  float* csum13 = hstat + 16 * 4096;
  {
    GemmArgs a = {x, wq, wk, wv, wo, Q, Kf, Vf, hstat, csum13, nullptr, w1, w3, w2};
    gemm_kernel<MODE_QKV, 64, 64, 512, 4><<<dim3(32, 11), 256, 0, stream>>>(a);
  }
  attn_kernel<<<dim3(256), 1024, 0, stream>>>(Q, Kf, Vf, O);
  {
    GemmArgs a = {O, wo, nullptr, nullptr, x, Hb, nullptr, nullptr, hstat, nullptr, nullptr, nullptr, nullptr, nullptr};
    gemm_kernel<MODE_RES, 64, 64, 512, 4><<<dim3(32, 8), 256, 0, stream>>>(a);
  }
  {
    GemmArgs a = {Hb, w1, w3, nullptr, nullptr, ACT, nullptr, nullptr, hstat, csum13, nullptr, nullptr, nullptr, nullptr};
    gemm_kernel<MODE_FFN1, 128, 64, 512, 2, 0, 4><<<dim3(16, 43), 256, 0, stream>>>(a);
  }
  {
    GemmArgs a = {ACT, w2, nullptr, nullptr, Hb, out, nullptr, nullptr, nullptr, nullptr, nullptr, nullptr, nullptr, nullptr};
    gemm_kernel<MODE_RES, 64, 64, 1376, 4><<<dim3(32, 8), 256, 0, stream>>>(a);
  }
}
```

```cpp
#include <hip/hip_runtime.h>
#include <hip/hip_bf16.h>
#include <stdint.h>
#include <type_traits>


typedef short bf16x8 __attribute__((ext_vector_type(8)));
typedef short s16x4 __attribute__((ext_vector_type(4)));
typedef float f32x16 __attribute__((ext_vector_type(16)));
typedef float f32x4 __attribute__((ext_vector_type(4)));
typedef float f32x2 __attribute__((ext_vector_type(2)));
typedef __bf16 bf16x2v __attribute__((ext_vector_type(2)));
typedef unsigned u32x4 __attribute__((ext_vector_type(4)));
typedef unsigned u32x2 __attribute__((ext_vector_type(2)));
typedef __attribute__((address_space(3))) s16x4 lds_s16x4;
typedef unsigned short u16;

#define DI __device__ __forceinline__
#define MFMA32(a, b, c) __builtin_amdgcn_mfma_f32_32x32x16_bf16((a), (b), (c), 0, 0, 0)

constexpr int S_ = 2048, D_ = 512, F_ = 1376;

DI unsigned pk2(float a, float b) {
  f32x2 v = {a, b};
  bf16x2v r = __builtin_convertvector(v, bf16x2v);
  return __builtin_bit_cast(unsigned, r);
}
DI float bflo(unsigned u) { return __uint_as_float(u << 16); }
DI float bfhi(unsigned u) { return __uint_as_float(u & 0xffff0000u); }
DI float dot2bf(unsigned a, unsigned b, float c) {
  return __builtin_amdgcn_fdot2_f32_bf16(__builtin_bit_cast(bf16x2v, a), __builtin_bit_cast(bf16x2v, b), c, false);
}

DI void glds16(const void* gsrc, unsigned lds_dst) {
  unsigned keep;
  asm volatile("s_mov_b32 %0, m0\n\ts_mov_b32 m0, %2\n\ts_nop 0\n\tglobal_load_lds_dwordx4 %1, off\n\ts_mov_b32 m0, %0"
               : "=&s"(keep) : "v"(gsrc), "s"(lds_dst) : "memory");
}

enum { MODE_QKV = 0, MODE_RES = 1, MODE_FFN1 = 2 };

template <int BN>
DI unsigned wswz(unsigned row) {
  if constexpr (BN == 128) return ((row & 3) << 2) | ((row >> 2) & 3);
  else return ((row >> 1) & 1) << 2;
}

struct GemmArgs {
  const u16* A;
  const u16* W0;
  const u16* W1;
  const u16* W2;
  const u16* res;
  u16* out0;
  u16* out1;
  u16* out2;
  float* hstat;
  float* csum13;
  float* kmax;
  const u16* w1;
  const u16* w3;
  const u16* out2src;
};

template <int MODE, int BM, int BN, int KTOT, int NST, int DBG = 0, int WGM = 2>
__global__ __launch_bounds__(256) void gemm_kernel(GemmArgs args) {
  constexpr int WGN = 4 / WGM, WMT = BM / (32 * WGM), WNT = BN / (32 * WGN);
  constexpr int XB = BM * 128, WB = 64 * BN * 2, STB = XB + WB;
  constexpr int NXI = BM / 32, NWI = BN / 32, G = NXI + NWI;
  constexpr int NKT = (KTOT + 63) / 64;
  constexpr bool LN = (MODE != MODE_RES);
  constexpr bool LNSTAT = (MODE == MODE_QKV);
  constexpr int LDA = (MODE == MODE_RES) ? KTOT : 512;
  constexpr int LDW = (MODE == MODE_FFN1) ? F_ : 512;
  __shared__ __attribute__((aligned(16))) char sm[NST * STB];
  const int tid = threadIdx.x, lane = tid & 63, wid = tid >> 6;
  const int wm = wid % WGM, wn = wid / WGM, h = lane >> 5;
  const int row0 = blockIdx.x * BM, cb = blockIdx.y;
  if constexpr (MODE == MODE_QKV) {
    if (cb >= 9) {
      const int j = blockIdx.x + (cb - 9) * 32;
      if (j < 22) {
        const int cc = tid & 15, rg = tid >> 4;
        const int col = j * 128 + cc * 8;
        float a[8] = {};
        if (col < 2 * F_) {
          const u16* wsrc = (col < F_ ? args.w1 + col : args.w3 + (col - F_)) + (size_t)rg * 32 * F_;
          u32x4 v[32];
#pragma unroll
          for (int r = 0; r < 32; ++r) v[r] = *(const u32x4*)(wsrc + (size_t)r * F_);
#pragma unroll
          for (int r = 0; r < 32; ++r)
#pragma unroll
            for (int e = 0; e < 4; ++e) { a[2 * e] += bflo(v[r][e]); a[2 * e + 1] += bfhi(v[r][e]); }
        }
        float* red = (float*)sm;
#pragma unroll
        for (int e = 0; e < 8; ++e) red[rg * 128 + cc * 8 + e] = a[e];
        __syncthreads();
        if (tid < 128 && j * 128 + tid < 2 * F_) {
          float t = 0.f;
#pragma unroll
          for (int g = 0; g < 16; ++g) t += red[g * 128 + tid];
          args.csum13[j * 128 + tid] = t;
        }
      } else {
        const size_t base = ((size_t)(j - 22) * 16 * 256 + tid) * 16;
        u32x4 t[16];
#pragma unroll
        for (int i = 0; i < 16; ++i) {
          const size_t off = base + (size_t)i * 4096;
          const char* pp = off < 524288 ? (const char*)args.res + off
                         : (off < 524288 + (size_t)F_ * 1024 ? (const char*)args.out2src + (off - 524288) : (const char*)args.res);
          asm volatile("global_load_dwordx4 %0, %1, off" : "=v"(t[i]) : "v"(pp) : "memory");
        }
        asm volatile("s_waitcnt vmcnt(0)" ::: "memory");
#pragma unroll
        for (int i = 0; i < 16; ++i) asm volatile("" ::"v"(t[i]));
      }
      return;
    }
  }

  const u16* xp[NXI];
#pragma unroll
  for (int i = 0; i < NXI; ++i) {
    const int r = (i * 4 + wid) * 8 + (lane >> 3), c = (lane & 7) ^ ((r >> 1) & 7);
    xp[i] = args.A + (size_t)(row0 + r) * LDA + c * 8;
  }
  const u16* wp[NWI];
  int wrow[NWI], wld[NWI];
#pragma unroll
  for (int i = 0; i < NWI; ++i) {
    const int p = (i * 4 + wid) * 64 + lane;
    const int r = p / (BN / 8), c = (p % (BN / 8)) ^ wswz<BN>(r);
    wrow[i] = r;
    if constexpr (MODE == MODE_QKV) {
      if (cb < 8) { wp[i] = args.W0 + cb * 64 + c * 8; wld[i] = 512; }
      else if (c < 2) { wp[i] = args.W1 + c * 8; wld[i] = 16; }
      else { wp[i] = args.W2 + (c & 1) * 8; wld[i] = 16; }
    } else if constexpr (MODE == MODE_RES) {
      wp[i] = args.W0 + cb * BN + c * 8; wld[i] = 512;
    } else {
      const int c32 = c >> 2;
      int f = cb * (BN / 2) + (c32 >> 1) * 32 + (c & 3) * 8;
      if (f >= F_) f = 0;
      wp[i] = ((c32 & 1) ? args.W1 : args.W0) + f; wld[i] = F_;
    }
  }
  const unsigned lds0 = (unsigned)(size_t)sm + (unsigned)__builtin_amdgcn_readfirstlane(wid) * 1024u;
  auto issue = [&](int kt, int buf) {
    const unsigned base = lds0 + buf * STB;
#pragma unroll
    for (int i = 0; i < NXI; ++i) glds16(xp[i] + kt * 64, base + i * 4096);
#pragma unroll
    for (int i = 0; i < NWI; ++i) {
      int k = kt * 64 + wrow[i];
      if constexpr (KTOT % 64 != 0) k = k < KTOT ? k : KTOT - 1;
      glds16(wp[i] + (size_t)k * wld[i], base + XB + i * 4096);
    }
  };

  f32x16 acc[WNT][WMT] = {};
  float sx = 0.f, sxx = 0.f, cs = 0.f;
  const int wmu = __builtin_amdgcn_readfirstlane(wm), wnu = __builtin_amdgcn_readfirstlane(wn);
  unsigned xoff[WMT];
#pragma unroll
  for (int t = 0; t < WMT; ++t) xoff[t] = (unsigned)((wm * WMT + t) * 32 + (lane & 31)) * 128u;
  const unsigned xsw = ((lane & 31) >> 1) & 7;
  const unsigned tq = (lane & 15) >> 2, tp = lane & 3, tblk = (lane >> 4) & 1;

  constexpr int PD = NST - 1;
#pragma unroll
  for (int i = 0; i < PD; ++i)
    if (i < NKT) issue(i, i);
  for (int kt = 0; kt < NKT; ++kt) {
    const int rem = NKT - 1 - kt;
    if (rem >= PD - 1) asm volatile("s_waitcnt vmcnt(%0)" ::"n"((PD - 1) * G) : "memory");
    else if (PD >= 3 && rem == 1) asm volatile("s_waitcnt vmcnt(%0)" ::"n"(G) : "memory");
    else if (PD >= 4 && rem == 2) asm volatile("s_waitcnt vmcnt(%0)" ::"n"(2 * G) : "memory");
    else asm volatile("s_waitcnt vmcnt(0)" ::: "memory");
    __builtin_amdgcn_s_barrier();
    asm volatile("" ::: "memory");
    if (kt + PD < NKT && DBG != 2) issue(kt + PD, (kt + PD) % NST);
    const char* Xs = sm + (kt % NST) * STB;
    const char* Ws = Xs + XB;
    const int nks = (KTOT % 64 != 0 && kt == NKT - 1) ? (KTOT % 64) / 16 : 4;
    if (DBG != 1) {
      bf16x8 xb[4][WMT], wa[4][WNT];
#pragma unroll
      for (int ks = 0; ks < 4; ++ks) {
        if (ks < nks) {
#pragma unroll
          for (int t = 0; t < WMT; ++t) xb[ks][t] = *(const bf16x8*)(Xs + xoff[t] + (((2 * ks + h) ^ xsw) << 4));
#pragma unroll
          for (int t = 0; t < WNT; ++t) {
            const unsigned c = (wn * WNT + t) * 4 + 2 * tblk + (tp >> 1);
            const unsigned r0 = 16 * ks + 8 * h + tq, r1 = r0 + 4;
            s16x4 lo = __builtin_amdgcn_ds_read_tr16_b64_v4i16((lds_s16x4*)(Ws + r0 * (BN * 2) + ((c ^ wswz<BN>(r0)) << 4) + 8 * (tp & 1)));
            s16x4 hi = __builtin_amdgcn_ds_read_tr16_b64_v4i16((lds_s16x4*)(Ws + r1 * (BN * 2) + ((c ^ wswz<BN>(r1)) << 4) + 8 * (tp & 1)));
            wa[ks][t] = __builtin_shufflevector(lo, hi, 0, 1, 2, 3, 4, 5, 6, 7);
          }
        }
      }
      __builtin_amdgcn_sched_barrier(0);
#pragma unroll
      for (int ks = 0; ks < 4; ++ks) {
        if (ks < nks) {
#pragma unroll
          for (int tn = 0; tn < WNT; ++tn)
#pragma unroll
            for (int tm = 0; tm < WMT; ++tm) acc[tn][tm] = MFMA32(wa[ks][tn], xb[ks][tm], acc[tn][tm]);
          if constexpr (LNSTAT) {
            static_assert(WMT == 1 && WNT == 1, "in-kernel LN stats assume a 64x64 tile");
            if (wmu == wnu) {
              const u32x4 u = __builtin_bit_cast(u32x4, xb[ks][0]);
#pragma unroll
              for (int j = 0; j < 4; ++j) { sx = dot2bf(u[j], 0x3f803f80u, sx); sxx = dot2bf(u[j], u[j], sxx); }
            } else {
              const u32x4 u = __builtin_bit_cast(u32x4, wa[ks][0]);
#pragma unroll
              for (int j = 0; j < 4; ++j) cs = dot2bf(u[j], 0x3f803f80u, cs);
            }
          }
        }
      }
    }
  }

  float mean[WMT], rstd[WMT];
  if constexpr (LNSTAT) {
    __syncthreads();
    float* csum_s = (float*)sm;
    float* stat_s = (float*)sm + BN;
    if (wmu == wnu) {
      const float s1 = sx + __shfl_xor(sx, 32), s2 = sxx + __shfl_xor(sxx, 32);
      const float mu = s1 * (1.f / 512);
      const float var = fmaxf(s2 * (1.f / 512) - mu * mu, 0.f);
      if (h == 0) *(f32x2*)(stat_s + (wm * 32 + lane) * 2) = (f32x2){mu, 1.f / sqrtf(var + 1e-5f)};
    } else {
      const float c = cs + __shfl_xor(cs, 32);
      if (h == 0) csum_s[wn * 32 + lane] = c;
    }
    __syncthreads();
    const f32x2 st = *(const f32x2*)(stat_s + (wm * 32 + (lane & 31)) * 2);
    mean[0] = st[0]; rstd[0] = st[1];
#pragma unroll
    for (int g = 0; g < 4; ++g) {
      const f32x4 c4 = *(const f32x4*)(csum_s + wn * 32 + 8 * g + 4 * h);
#pragma unroll
      for (int e = 0; e < 4; ++e) acc[0][0][4 * g + e] = rstd[0] * (acc[0][0][4 * g + e] - mean[0] * c4[e]);
    }
  } else if constexpr (LN) {
#pragma unroll
    for (int t = 0; t < WMT; ++t) {
      f32x2 st = {0.f, 0.f};
#pragma unroll
      for (int j = 0; j < 8; ++j) st += *(const f32x2*)(args.hstat + ((size_t)j * S_ + row0 + (wm * WMT + t) * 32 + (lane & 31)) * 2);
      mean[t] = st[0] * (1.f / 512);
      const float var = fmaxf(st[1] * (1.f / 512) - mean[t] * mean[t], 0.f);
      rstd[t] = 1.f / sqrtf(var + 1e-5f);
    }
    const int fb = min(cb * (BN / 2) + wn * 32, F_ - 32);
#pragma unroll
    for (int tn = 0; tn < WNT; ++tn) {
#pragma unroll
      for (int g = 0; g < 4; ++g) {
        const f32x4 c4 = *(const f32x4*)(args.csum13 + tn * F_ + fb + 8 * g + 4 * h);
#pragma unroll
        for (int tm = 0; tm < WMT; ++tm)
#pragma unroll
          for (int e = 0; e < 4; ++e) acc[tn][tm][4 * g + e] = rstd[tm] * (acc[tn][tm][4 * g + e] - mean[tm] * c4[e]);
      }
    }
  }

  if constexpr (MODE == MODE_QKV) {
    if (cb == 8) {
      if (wn == 0) {
        const int m = row0 + wm * 32 + (lane & 31);
        const f32x16& a = acc[0][0];
#pragma unroll
        for (int g = 0; g < 2; ++g) {
          u32x2 o = {pk2(a[4 * g], a[4 * g + 1]), pk2(a[4 * g + 2], a[4 * g + 3])};
          *(u32x2*)(args.out1 + (size_t)m * 16 + 8 * g + 4 * h) = o;
        }
        const int kt = m >> 5, ks = (m >> 4) & 1, ka = (m >> 3) & 1, kh = (m >> 2) & 1, kb = m & 3;
        u16* vbase = args.out2 + (size_t)((((kt * 2 + ks) * 2 + kh) * 16) * 8) + 4 * ka + kb;
#pragma unroll
        for (int g = 2; g < 4; ++g)
#pragma unroll
          for (int e = 0; e < 4; ++e) vbase[(8 * (g - 2) + 4 * h + e) * 8] = (u16)(pk2(a[4 * g + e], 0.f) & 0xffffu);
      }
      return;
    }
  }
  constexpr int NC = (MODE == MODE_FFN1) ? BN / 2 : BN;
  constexpr int SSTR = NC + 4;
  static_assert(BM * SSTR * 4 <= NST * STB, "staging tile must fit");
  float* stg = (float*)sm;
  __syncthreads();
#pragma unroll
  for (int tm = 0; tm < WMT; ++tm) {
    float* srow = stg + ((wm * WMT + tm) * 32 + (lane & 31)) * SSTR + 4 * h;
    if constexpr (MODE == MODE_FFN1) {
#pragma unroll
      for (int g = 0; g < 4; ++g) {
        f32x4 r;
#pragma unroll
        for (int e = 0; e < 4; ++e) {
          const unsigned gu = pk2(acc[0][tm][4 * g + e], acc[1][tm][4 * g + e]);
          const float gt = bflo(gu), up = bfhi(gu);
          const float sg = gt * __builtin_amdgcn_rcpf(1.f + __builtin_amdgcn_exp2f(gt * -1.4426950408889634f));
          r[e] = bflo(pk2(sg, 0.f)) * up;
        }
        *(f32x4*)(srow + wn * 32 + 8 * g) = r;
      }
    } else {
#pragma unroll
      for (int tn = 0; tn < WNT; ++tn)
#pragma unroll
        for (int g = 0; g < 4; ++g) {
          const f32x16& a = acc[tn][tm];
          *(f32x4*)(srow + (wn * WNT + tn) * 32 + 8 * g) = (f32x4){a[4 * g], a[4 * g + 1], a[4 * g + 2], a[4 * g + 3]};
        }
    }
  }
  __syncthreads();
  constexpr int CPR = NC / 8;
  constexpr int RPP = 256 / CPR;
  const int cc = tid % CPR, rr = tid / CPR;
#pragma unroll
  for (int pss = 0; pss < BM / RPP; ++pss) {
    const int rl = pss * RPP + rr, m = row0 + rl;
    f32x4 v0 = *(const f32x4*)(stg + rl * SSTR + cc * 8), v1 = *(const f32x4*)(stg + rl * SSTR + cc * 8 + 4);
    if constexpr (MODE == MODE_QKV) {
      const float qs = 0.25f * 1.4426950408889634f;
      v0 *= qs; v1 *= qs;
      u32x4 o = {pk2(v0[0], v0[1]), pk2(v0[2], v0[3]), pk2(v1[0], v1[1]), pk2(v1[2], v1[3])};
      *(u32x4*)(args.out0 + (size_t)m * D_ + cb * BN + cc * 8) = o;
    } else if constexpr (MODE == MODE_RES) {
      const size_t off = (size_t)m * D_ + cb * BN + cc * 8;
      const u32x4 rv = *(const u32x4*)(args.res + off);
      u32x4 o = {pk2(v0[0] + bflo(rv[0]), v0[1] + bfhi(rv[0])), pk2(v0[2] + bflo(rv[1]), v0[3] + bfhi(rv[1])),
                 pk2(v1[0] + bflo(rv[2]), v1[1] + bfhi(rv[2])), pk2(v1[2] + bflo(rv[3]), v1[3] + bfhi(rv[3]))};
      *(u32x4*)(args.out0 + off) = o;
      if constexpr (KTOT == 512) {
        float s1 = 0.f, s2 = 0.f;
#pragma unroll
        for (int j = 0; j < 4; ++j) { s1 = dot2bf(o[j], 0x3f803f80u, s1); s2 = dot2bf(o[j], o[j], s2); }
#pragma unroll
        for (int d = 1; d < CPR; d <<= 1) { s1 += __shfl_xor(s1, d); s2 += __shfl_xor(s2, d); }
        if (cc == 0) *(f32x2*)(args.hstat + ((size_t)cb * S_ + m) * 2) = (f32x2){s1, s2};
      }
    } else {
      const int f = cb * NC + cc * 8;
      if (f < F_) {
        u32x4 o = {pk2(v0[0], v0[1]), pk2(v0[2], v0[3]), pk2(v1[0], v1[1]), pk2(v1[2], v1[3])};
        *(u32x4*)(args.out0 + (size_t)m * F_ + f) = o;
      }
    }
  }
}

template <bool EXACT>
DI void attn_step(f32x16& sc_, f32x16& sn_, f32x16& o, f32x16& negm, bf16x8& kn, const bf16x8 qf,
                  const char* kp, const char* vp0, int vstride, int vd, int t) {
  const bf16x8 v0 = *(const bf16x8*)(vp0 + t * vstride);
  const bf16x8 v1 = *(const bf16x8*)(vp0 + t * vstride + vd);
  sn_ = MFMA32(kn, qf, negm);
  kn = *(const bf16x8*)(kp + ((t + 2) & 31) * 1024);
  if constexpr (EXACT) {
    float m0 = fmaxf(fmaxf(sc_[0], sc_[1]), sc_[2]), m1 = fmaxf(fmaxf(sc_[3], sc_[4]), sc_[5]);
    float m2 = fmaxf(fmaxf(sc_[6], sc_[7]), sc_[8]), m3 = fmaxf(fmaxf(sc_[9], sc_[10]), sc_[11]);
    float m4 = fmaxf(fmaxf(sc_[12], sc_[13]), sc_[14]);
    float mx = fmaxf(fmaxf(fmaxf(m0, m1), sc_[15]), fmaxf(fmaxf(m2, m3), m4));
    if (__builtin_expect(__any(mx > 8.f), 0)) {
      mx = fmaxf(mx, __shfl_xor(mx, 32));
      mx = fmaxf(mx, 0.f);
      const float sc = __builtin_amdgcn_exp2f(-mx);
      o *= sc; negm -= mx; sc_ -= mx; sn_ -= mx;
    }
  }
  u32x4 p0, p1;
#pragma unroll
  for (int i = 0; i < 4; ++i) {
    p0[i] = pk2(__builtin_amdgcn_exp2f(sc_[2 * i]), __builtin_amdgcn_exp2f(sc_[2 * i + 1]));
    p1[i] = pk2(__builtin_amdgcn_exp2f(sc_[8 + 2 * i]), __builtin_amdgcn_exp2f(sc_[9 + 2 * i]));
  }
  o = MFMA32(v0, __builtin_bit_cast(bf16x8, p0), o);
  o = MFMA32(v1, __builtin_bit_cast(bf16x8, p1), o);
}

constexpr int ATT_LDS = 131072 + 64 + 16384;
__global__ __launch_bounds__(1024) void attn_kernel(const u16* __restrict__ Q, const u16* __restrict__ Kf,
                                                    const u16* __restrict__ Vf, u16* __restrict__ O,
                                                    const u16* __restrict__ pf0, const u16* __restrict__ pf1,
                                                    const u16* __restrict__ pf2) {
  __shared__ __attribute__((aligned(16))) char sm[ATT_LDS];
  const int tid = threadIdx.x, lane = tid & 63, wid = tid >> 6;
  const int widu = __builtin_amdgcn_readfirstlane(wid);
  const int h = lane >> 5, r = lane & 31;
  const int q = blockIdx.x * 8 + (wid & 7), half = wid >> 3;
  bf16x8 qf;
  asm volatile("global_load_dwordx4 %0, %1, off" : "=v"(qf) : "v"(Q + (size_t)q * D_ + r * 16 + h * 8) : "memory");
  {
    const unsigned lds0 = (unsigned)(size_t)sm;
    const int hf = (widu >> 2) & 1, isv = widu >> 3, pc = widu & 3;
    const unsigned off0 = isv * 65536 + hf * 32768 + pc * 2048;
    const char* src = (const char*)(isv ? Vf : Kf) + hf * 32768 + pc * 2048 + lane * 16;
#pragma unroll
    for (int c = 0; c < 4; ++c) {
      glds16(src + c * 8192, lds0 + off0 + c * 8192);
      glds16(src + c * 8192 + 1024, lds0 + off0 + c * 8192 + 1024);
    }
    if (tid < 8) ((unsigned*)(sm + 131072))[tid] = tid < 4 ? 0x3f803f80u : 0u;
  }
  asm volatile("s_waitcnt vmcnt(6) lgkmcnt(0)" : "+v"(qf)::"memory");
  __builtin_amdgcn_s_barrier();
  asm volatile("" ::: "memory");
  const char* kp = sm + half * 32768 + r * 32 + h * 16;
  const char* vp0 = sm + (r < 16 ? 65536 + half * 32768 + h * 256 + r * 16 : (r == 16 ? 131072 : 131088));
  const int vd = r < 16 ? 512 : 0;
  const int vstride = r < 16 ? 1024 : 0;
  f32x16 negm, sA, sB = {};
  {
    f32x16 z = {};
    f32x16 s0 = MFMA32(*(const bf16x8*)kp, qf, z);
    float mx = s0[0];
#pragma unroll
    for (int i = 1; i < 16; ++i) mx = fmaxf(mx, s0[i]);
    mx = fmaxf(mx, __shfl_xor(mx, 32));
    negm = (f32x16)(-mx);
    sA = s0 + negm;
  }
  f32x16 o = {};
  bf16x8 kn = *(const bf16x8*)(kp + 1024);
#define ATT_SEG(T0, T1)                                                            \
  for (int t = T0; t < T1; t += 2) {                                               \
    attn_step<false>(sA, sB, o, negm, kn, qf, kp, vp0, vstride, vd, t);            \
    attn_step<false>(sB, sA, o, negm, kn, qf, kp, vp0, vstride, vd, t + 1);        \
  }
#define ATT_SYNC(N)                                                                \
  asm volatile("s_waitcnt vmcnt(" #N ")" ::: "memory");                            \
  __builtin_amdgcn_s_barrier();                                                    \
  asm volatile("" ::: "memory");
  ATT_SEG(0, 6)
  ATT_SYNC(4)
  ATT_SEG(6, 14)
  ATT_SYNC(2)
  ATT_SEG(14, 22)
  ATT_SYNC(0)
  {
    const int sl = blockIdx.x >> 3;
    const size_t o0 = (size_t)sl * 16384 + (size_t)wid * 1024 + lane * 16;
    const size_t o1 = (size_t)sl * 44032 + lane * 16;
    const unsigned scr = (unsigned)(size_t)sm + 131136u + (unsigned)widu * 1024u;
    glds16((const char*)pf0 + o0, scr);
#pragma unroll
    for (int i = 0; i < 3; ++i) {
      const int pc = widu + 16 * i;
      if (pc < 43) {
        glds16((const char*)pf1 + o1 + pc * 1024, scr);
        glds16((const char*)pf2 + o1 + pc * 1024, scr);
      }
    }
  }
  ATT_SEG(22, 32)
  asm volatile("s_waitcnt vmcnt(0)" ::: "memory");
  {
    const float l = __shfl(o[8], r);
    if (__any(!(l < 1e30f))) {
      o = (f32x16){};
      sA = MFMA32(*(const bf16x8*)kp, qf, negm);
      kn = *(const bf16x8*)(kp + 1024);
      for (int t = 0; t < 32; t += 2) {
        attn_step<true>(sA, sB, o, negm, kn, qf, kp, vp0, vstride, vd, t);
        attn_step<true>(sB, sA, o, negm, kn, qf, kp, vp0, vstride, vd, t + 1);
      }
    }
  }
  __syncthreads();
  float* ex = (float*)sm + (wid & 7) * (64 * 10);
  if (half == 1) {
#pragma unroll
    for (int i = 0; i < 9; ++i) ex[i * 64 + lane] = o[i];
    ex[9 * 64 + lane] = negm[0];
  }
  __syncthreads();
  if (half == 0) {
    const float nm1 = ex[9 * 64 + lane], nm0 = negm[0];
    const float nm = fminf(nm0, nm1);
    const float a0 = __builtin_amdgcn_exp2f(nm - nm0), a1 = __builtin_amdgcn_exp2f(nm - nm1);
#pragma unroll
    for (int i = 0; i < 9; ++i) o[i] = o[i] * a0 + ex[i * 64 + lane] * a1;
    const float l = __shfl(o[8], r);
    const float rl = 1.f / l;
    u32x2 oa = {pk2(o[0] * rl, o[1] * rl), pk2(o[2] * rl, o[3] * rl)};
    u32x2 ob = {pk2(o[4] * rl, o[5] * rl), pk2(o[6] * rl, o[7] * rl)};
    *(u32x2*)(O + (size_t)q * D_ + r * 16 + 4 * h) = oa;
    *(u32x2*)(O + (size_t)q * D_ + r * 16 + 8 + 4 * h) = ob;
  }
}

extern "C" void kernel_launch(void* const* d_in, const int* in_sizes, int n_in, void* d_out, int out_size,
                              void* d_ws, size_t ws_size, hipStream_t stream) {
  const u16* x = (const u16*)d_in[0];
  const u16* wq = (const u16*)d_in[1];
  const u16* wk = (const u16*)d_in[2];
  const u16* wv = (const u16*)d_in[3];
  const u16* wo = (const u16*)d_in[4];
  const u16* w1 = (const u16*)d_in[6];
  const u16* w3 = (const u16*)d_in[7];
  const u16* w2 = (const u16*)d_in[8];
  u16* out = (u16*)d_out;
  char* ws = (char*)d_ws;
  u16* Q = (u16*)(ws);
  u16* Kf = (u16*)(ws + (2u << 20));
  u16* Vf = (u16*)(ws + (2u << 20) + 65536);
  u16* O = (u16*)(ws + (3u << 20));
  u16* Hb = (u16*)(ws + (5u << 20));
  u16* ACT = (u16*)(ws + (7u << 20));
  float* hstat = (float*)(ws + (2u << 20) + 131072);
  float* csum13 = hstat + 16 * 4096;
  {
    GemmArgs a = {x, wq, wk, wv, wo, Q, Kf, Vf, hstat, csum13, nullptr, w1, w3, w2};
    gemm_kernel<MODE_QKV, 64, 64, 512, 4><<<dim3(32, 11), 256, 0, stream>>>(a);
  }
  attn_kernel<<<dim3(256), 1024, 0, stream>>>(Q, Kf, Vf, O, wo, w1, w3);
  {
    GemmArgs a = {O, wo, nullptr, nullptr, x, Hb, nullptr, nullptr, hstat, nullptr, nullptr, nullptr, nullptr, nullptr};
    gemm_kernel<MODE_RES, 64, 64, 512, 4><<<dim3(32, 8), 256, 0, stream>>>(a);
  }
  {
    GemmArgs a = {Hb, w1, w3, nullptr, nullptr, ACT, nullptr, nullptr, hstat, csum13, nullptr, nullptr, nullptr, nullptr};
    gemm_kernel<MODE_FFN1, 128, 64, 512, 2, 0, 4><<<dim3(16, 43), 256, 0, stream>>>(a);
  }
  {
    GemmArgs a = {ACT, w2, nullptr, nullptr, Hb, out, nullptr, nullptr, nullptr, nullptr, nullptr, nullptr, nullptr, nullptr};
    gemm_kernel<MODE_RES, 64, 64, 1376, 4><<<dim3(32, 8), 256, 0, stream>>>(a);
  }
}
```

```cpp
#include <hip/hip_runtime.h>
#include <hip/hip_bf16.h>
#include <stdint.h>
#include <type_traits>


typedef short bf16x8 __attribute__((ext_vector_type(8)));
typedef short s16x4 __attribute__((ext_vector_type(4)));
typedef float f32x16 __attribute__((ext_vector_type(16)));
typedef float f32x4 __attribute__((ext_vector_type(4)));
typedef float f32x2 __attribute__((ext_vector_type(2)));
typedef __bf16 bf16x2v __attribute__((ext_vector_type(2)));
typedef unsigned u32x4 __attribute__((ext_vector_type(4)));
typedef unsigned u32x2 __attribute__((ext_vector_type(2)));
typedef __attribute__((address_space(3))) s16x4 lds_s16x4;
typedef unsigned short u16;

#define DI __device__ __forceinline__
#define MFMA32(a, b, c) __builtin_amdgcn_mfma_f32_32x32x16_bf16((a), (b), (c), 0, 0, 0)

constexpr int S_ = 2048, D_ = 512, F_ = 1376;

DI unsigned pk2(float a, float b) {
  f32x2 v = {a, b};
  bf16x2v r = __builtin_convertvector(v, bf16x2v);
  return __builtin_bit_cast(unsigned, r);
}
DI float bflo(unsigned u) { return __uint_as_float(u << 16); }
DI float bfhi(unsigned u) { return __uint_as_float(u & 0xffff0000u); }
DI float dot2bf(unsigned a, unsigned b, float c) {
  return __builtin_amdgcn_fdot2_f32_bf16(__builtin_bit_cast(bf16x2v, a), __builtin_bit_cast(bf16x2v, b), c, false);
}

DI void glds16(const void* gsrc, unsigned lds_dst) {
  unsigned keep;
  asm volatile("s_mov_b32 %0, m0\n\ts_mov_b32 m0, %2\n\ts_nop 0\n\tglobal_load_lds_dwordx4 %1, off\n\ts_mov_b32 m0, %0"
               : "=&s"(keep) : "v"(gsrc), "s"(lds_dst) : "memory");
}

enum { MODE_QKV = 0, MODE_RES = 1, MODE_FFN1 = 2 };

template <int BN>
DI unsigned wswz(unsigned row) {
  if constexpr (BN == 128) return ((row & 3) << 2) | ((row >> 2) & 3);
  else return ((row >> 1) & 1) << 2;
}

struct GemmArgs {
  const u16* A;
  const u16* W0;
  const u16* W1;
  const u16* W2;
  const u16* res;
  u16* out0;
  u16* out1;
  u16* out2;
  float* hstat;
  float* csum13;
  float* kmax;
  const u16* w1;
  const u16* w3;
  const u16* out2src;
};

template <int MODE, int BM, int BN, int KTOT, int NST, int DBG = 0, int WGM = 2, int NW = 4>
__global__ __launch_bounds__(NW * 64) void gemm_kernel(GemmArgs args) {
  constexpr int WGN = NW / WGM, WMT = BM / (32 * WGM), WNT = BN / (32 * WGN);
  constexpr int XB = BM * 128, WB = 64 * BN * 2, STB = XB + WB;
  constexpr int NXI = BM / (8 * NW), NWI = BN / (8 * NW), G = NXI + NWI;
  constexpr int NKT = (KTOT + 63) / 64;
  constexpr bool LN = (MODE != MODE_RES);
  constexpr bool LNSTAT = (MODE == MODE_QKV);
  constexpr int LDA = (MODE == MODE_RES) ? KTOT : 512;
  constexpr int LDW = (MODE == MODE_FFN1) ? F_ : 512;
  __shared__ __attribute__((aligned(16))) char sm[NST * STB];
  const int tid = threadIdx.x, lane = tid & 63, wid = tid >> 6;
  const int wm = wid % WGM, wn = wid / WGM, h = lane >> 5;
  const int row0 = blockIdx.x * BM, cb = blockIdx.y;
  if constexpr (MODE == MODE_QKV) {
    if (cb >= 9) {
      const int j = blockIdx.x + (cb - 9) * 32;
      if (j < 22) {
        const int cc = tid & 15, rg = tid >> 4;
        const int col = j * 128 + cc * 8;
        float a[8] = {};
        if (col < 2 * F_) {
          const u16* wsrc = (col < F_ ? args.w1 + col : args.w3 + (col - F_)) + (size_t)rg * 32 * F_;
          u32x4 v[32];
#pragma unroll
          for (int r = 0; r < 32; ++r) v[r] = *(const u32x4*)(wsrc + (size_t)r * F_);
#pragma unroll
          for (int r = 0; r < 32; ++r)
#pragma unroll
            for (int e = 0; e < 4; ++e) { a[2 * e] += bflo(v[r][e]); a[2 * e + 1] += bfhi(v[r][e]); }
        }
        float* red = (float*)sm;
#pragma unroll
        for (int e = 0; e < 8; ++e) red[rg * 128 + cc * 8 + e] = a[e];
        __syncthreads();
        if (tid < 128 && j * 128 + tid < 2 * F_) {
          float t = 0.f;
#pragma unroll
          for (int g = 0; g < 16; ++g) t += red[g * 128 + tid];
          args.csum13[j * 128 + tid] = t;
        }
      } else {
        const size_t base = ((size_t)(j - 22) * 16 * 256 + tid) * 16;
        u32x4 t[16];
#pragma unroll
        for (int i = 0; i < 16; ++i) {
          const size_t off = base + (size_t)i * 4096;
          const char* pp = off < 524288 ? (const char*)args.res + off
                         : (off < 524288 + (size_t)F_ * 1024 ? (const char*)args.out2src + (off - 524288) : (const char*)args.res);
          asm volatile("global_load_dwordx4 %0, %1, off" : "=v"(t[i]) : "v"(pp) : "memory");
        }
        asm volatile("s_waitcnt vmcnt(0)" ::: "memory");
#pragma unroll
        for (int i = 0; i < 16; ++i) asm volatile("" ::"v"(t[i]));
      }
      return;
    }
  }

  const u16* xp[NXI];
#pragma unroll
  for (int i = 0; i < NXI; ++i) {
    const int r = (i * NW + wid) * 8 + (lane >> 3), c = (lane & 7) ^ ((r >> 1) & 7);
    xp[i] = args.A + (size_t)(row0 + r) * LDA + c * 8;
  }
  const u16* wp[NWI];
  int wrow[NWI], wld[NWI];
#pragma unroll
  for (int i = 0; i < NWI; ++i) {
    const int p = (i * NW + wid) * 64 + lane;
    const int r = p / (BN / 8), c = (p % (BN / 8)) ^ wswz<BN>(r);
    wrow[i] = r;
    if constexpr (MODE == MODE_QKV) {
      if (cb < 8) { wp[i] = args.W0 + cb * 64 + c * 8; wld[i] = 512; }
      else if (c < 2) { wp[i] = args.W1 + c * 8; wld[i] = 16; }
      else { wp[i] = args.W2 + (c & 1) * 8; wld[i] = 16; }
    } else if constexpr (MODE == MODE_RES) {
      wp[i] = args.W0 + cb * BN + c * 8; wld[i] = 512;
    } else {
      const int c32 = c >> 2;
      int f = cb * (BN / 2) + (c32 >> 1) * 32 + (c & 3) * 8;
      if (f >= F_) f = 0;
      wp[i] = ((c32 & 1) ? args.W1 : args.W0) + f; wld[i] = F_;
    }
  }
  const unsigned lds0 = (unsigned)(size_t)sm + (unsigned)__builtin_amdgcn_readfirstlane(wid) * 1024u;
  auto issue = [&](int kt, int buf) {
    const unsigned base = lds0 + buf * STB;
#pragma unroll
    for (int i = 0; i < NXI; ++i) glds16(xp[i] + kt * 64, base + i * (NW * 1024));
#pragma unroll
    for (int i = 0; i < NWI; ++i) {
      int k = kt * 64 + wrow[i];
      if constexpr (KTOT % 64 != 0) k = k < KTOT ? k : KTOT - 1;
      glds16(wp[i] + (size_t)k * wld[i], base + XB + i * (NW * 1024));
    }
  };

  f32x16 acc[WNT][WMT] = {};
  float sx = 0.f, sxx = 0.f, cs = 0.f;
  const int wmu = __builtin_amdgcn_readfirstlane(wm), wnu = __builtin_amdgcn_readfirstlane(wn);
  unsigned xoff[WMT];
#pragma unroll
  for (int t = 0; t < WMT; ++t) xoff[t] = (unsigned)((wm * WMT + t) * 32 + (lane & 31)) * 128u;
  const unsigned xsw = ((lane & 31) >> 1) & 7;
  const unsigned tq = (lane & 15) >> 2, tp = lane & 3, tblk = (lane >> 4) & 1;

  constexpr int PD = NST - 1;
#pragma unroll
  for (int i = 0; i < PD; ++i)
    if (i < NKT) issue(i, i);
  constexpr int NC = (MODE == MODE_FFN1) ? BN / 2 : BN;
  constexpr int CPR = NC / 8;
  constexpr int RPP = NW * 64 / CPR;
  const int cc = tid % CPR, rr = tid / CPR;
  float mean[WMT], rstd[WMT];
  f32x4 c4[WNT][4];
  f32x2 hs[WMT][8];
  u32x4 resv[BM / RPP];
  if constexpr (MODE == MODE_FFN1) {
#pragma unroll
    for (int t = 0; t < WMT; ++t)
#pragma unroll
      for (int j = 0; j < 8; ++j) hs[t][j] = *(const f32x2*)(args.hstat + ((size_t)j * S_ + row0 + (wm * WMT + t) * 32 + (lane & 31)) * 2);
    const int fb = min(cb * (BN / 2) + wn * 32, F_ - 32);
#pragma unroll
    for (int tn = 0; tn < WNT; ++tn)
#pragma unroll
      for (int g = 0; g < 4; ++g) c4[tn][g] = *(const f32x4*)(args.csum13 + tn * F_ + fb + 8 * g + 4 * h);
  }
  if constexpr (MODE == MODE_RES) {
#pragma unroll
    for (int pss = 0; pss < BM / RPP; ++pss)
      resv[pss] = *(const u32x4*)(args.res + (size_t)(row0 + pss * RPP + rr) * D_ + cb * BN + cc * 8);
  }
  asm volatile("" ::: "memory");
  for (int kt = 0; kt < NKT; ++kt) {
    const int rem = NKT - 1 - kt;
    if (rem >= PD - 1) asm volatile("s_waitcnt vmcnt(%0)" ::"n"((PD - 1) * G) : "memory");
    else if (PD >= 3 && rem == 1) asm volatile("s_waitcnt vmcnt(%0)" ::"n"(G) : "memory");
    else if (PD >= 4 && rem == 2) asm volatile("s_waitcnt vmcnt(%0)" ::"n"(2 * G) : "memory");
    else asm volatile("s_waitcnt vmcnt(0)" ::: "memory");
    __builtin_amdgcn_s_barrier();
    asm volatile("" ::: "memory");
    if (kt + PD < NKT && DBG != 2) issue(kt + PD, (kt + PD) % NST);
    const char* Xs = sm + (kt % NST) * STB;
    const char* Ws = Xs + XB;
    const int nks = (KTOT % 64 != 0 && kt == NKT - 1) ? (KTOT % 64) / 16 : 4;
    if (DBG != 1) {
      bf16x8 xb[4][WMT], wa[4][WNT];
#pragma unroll
      for (int ks = 0; ks < 4; ++ks) {
        if (ks < nks) {
#pragma unroll
          for (int t = 0; t < WMT; ++t) xb[ks][t] = *(const bf16x8*)(Xs + xoff[t] + (((2 * ks + h) ^ xsw) << 4));
#pragma unroll
          for (int t = 0; t < WNT; ++t) {
            const unsigned c = (wn * WNT + t) * 4 + 2 * tblk + (tp >> 1);
            const unsigned r0 = 16 * ks + 8 * h + tq, r1 = r0 + 4;
            s16x4 lo = __builtin_amdgcn_ds_read_tr16_b64_v4i16((lds_s16x4*)(Ws + r0 * (BN * 2) + ((c ^ wswz<BN>(r0)) << 4) + 8 * (tp & 1)));
            s16x4 hi = __builtin_amdgcn_ds_read_tr16_b64_v4i16((lds_s16x4*)(Ws + r1 * (BN * 2) + ((c ^ wswz<BN>(r1)) << 4) + 8 * (tp & 1)));
            wa[ks][t] = __builtin_shufflevector(lo, hi, 0, 1, 2, 3, 4, 5, 6, 7);
          }
        }
      }
      __builtin_amdgcn_sched_barrier(0);
#pragma unroll
      for (int ks = 0; ks < 4; ++ks) {
        if (ks < nks) {
#pragma unroll
          for (int tn = 0; tn < WNT; ++tn)
#pragma unroll
            for (int tm = 0; tm < WMT; ++tm) acc[tn][tm] = MFMA32(wa[ks][tn], xb[ks][tm], acc[tn][tm]);
          if constexpr (LNSTAT) {
            static_assert(WMT == 1 && WNT == 1, "in-kernel LN stats assume a 64x64 tile");
            if (wmu == wnu) {
              const u32x4 u = __builtin_bit_cast(u32x4, xb[ks][0]);
#pragma unroll
              for (int j = 0; j < 4; ++j) { sx = dot2bf(u[j], 0x3f803f80u, sx); sxx = dot2bf(u[j], u[j], sxx); }
            } else {
              const u32x4 u = __builtin_bit_cast(u32x4, wa[ks][0]);
#pragma unroll
              for (int j = 0; j < 4; ++j) cs = dot2bf(u[j], 0x3f803f80u, cs);
            }
          }
        }
      }
    }
  }

  if constexpr (LNSTAT) {
    __syncthreads();
    float* csum_s = (float*)sm;
    float* stat_s = (float*)sm + BN;
    if (wmu == wnu) {
      const float s1 = sx + __shfl_xor(sx, 32), s2 = sxx + __shfl_xor(sxx, 32);
      const float mu = s1 * (1.f / 512);
      const float var = fmaxf(s2 * (1.f / 512) - mu * mu, 0.f);
      if (h == 0) *(f32x2*)(stat_s + (wm * 32 + lane) * 2) = (f32x2){mu, 1.f / sqrtf(var + 1e-5f)};
    } else {
      const float c = cs + __shfl_xor(cs, 32);
      if (h == 0) csum_s[wn * 32 + lane] = c;
    }
    __syncthreads();
    const f32x2 st = *(const f32x2*)(stat_s + (wm * 32 + (lane & 31)) * 2);
    mean[0] = st[0]; rstd[0] = st[1];
#pragma unroll
    for (int g = 0; g < 4; ++g) {
      const f32x4 c4 = *(const f32x4*)(csum_s + wn * 32 + 8 * g + 4 * h);
#pragma unroll
      for (int e = 0; e < 4; ++e) acc[0][0][4 * g + e] = rstd[0] * (acc[0][0][4 * g + e] - mean[0] * c4[e]);
    }
  } else if constexpr (LN) {
#pragma unroll
    for (int t = 0; t < WMT; ++t) {
      f32x2 st = hs[t][0];
#pragma unroll
      for (int j = 1; j < 8; ++j) st += hs[t][j];
      mean[t] = st[0] * (1.f / 512);
      const float var = fmaxf(st[1] * (1.f / 512) - mean[t] * mean[t], 0.f);
      rstd[t] = 1.f / sqrtf(var + 1e-5f);
    }
#pragma unroll
    for (int tn = 0; tn < WNT; ++tn)
#pragma unroll
      for (int g = 0; g < 4; ++g)
#pragma unroll
        for (int tm = 0; tm < WMT; ++tm)
#pragma unroll
          for (int e = 0; e < 4; ++e) acc[tn][tm][4 * g + e] = rstd[tm] * (acc[tn][tm][4 * g + e] - mean[tm] * c4[tn][g][e]);
  }

  if constexpr (MODE == MODE_QKV) {
    if (cb == 8) {
      if (wn == 0) {
        const int m = row0 + wm * 32 + (lane & 31);
        const f32x16& a = acc[0][0];
#pragma unroll
        for (int g = 0; g < 2; ++g) {
          u32x2 o = {pk2(a[4 * g], a[4 * g + 1]), pk2(a[4 * g + 2], a[4 * g + 3])};
          *(u32x2*)(args.out1 + (size_t)m * 16 + 8 * g + 4 * h) = o;
        }
        const int kt = m >> 5, ks = (m >> 4) & 1, ka = (m >> 3) & 1, kh = (m >> 2) & 1, kb = m & 3;
        u16* vbase = args.out2 + (size_t)((((kt * 2 + ks) * 2 + kh) * 16) * 8) + 4 * ka + kb;
#pragma unroll
        for (int g = 2; g < 4; ++g)
#pragma unroll
          for (int e = 0; e < 4; ++e) vbase[(8 * (g - 2) + 4 * h + e) * 8] = (u16)(pk2(a[4 * g + e], 0.f) & 0xffffu);
      }
      return;
    }
  }
  constexpr int SSTR = NC + 4;
  static_assert(BM * SSTR * 4 <= NST * STB, "staging tile must fit");
  float* stg = (float*)sm;
  __syncthreads();
#pragma unroll
  for (int tm = 0; tm < WMT; ++tm) {
    float* srow = stg + ((wm * WMT + tm) * 32 + (lane & 31)) * SSTR + 4 * h;
    if constexpr (MODE == MODE_FFN1) {
#pragma unroll
      for (int g = 0; g < 4; ++g) {
        f32x4 r;
#pragma unroll
        for (int e = 0; e < 4; ++e) {
          const unsigned gu = pk2(acc[0][tm][4 * g + e], acc[1][tm][4 * g + e]);
          const float gt = bflo(gu), up = bfhi(gu);
          const float sg = gt * __builtin_amdgcn_rcpf(1.f + __builtin_amdgcn_exp2f(gt * -1.4426950408889634f));
          r[e] = bflo(pk2(sg, 0.f)) * up;
        }
        *(f32x4*)(srow + wn * 32 + 8 * g) = r;
      }
    } else {
#pragma unroll
      for (int tn = 0; tn < WNT; ++tn)
#pragma unroll
        for (int g = 0; g < 4; ++g) {
          const f32x16& a = acc[tn][tm];
          *(f32x4*)(srow + (wn * WNT + tn) * 32 + 8 * g) = (f32x4){a[4 * g], a[4 * g + 1], a[4 * g + 2], a[4 * g + 3]};
        }
    }
  }
  __syncthreads();
#pragma unroll
  for (int pss = 0; pss < BM / RPP; ++pss) {
    const int rl = pss * RPP + rr, m = row0 + rl;
    f32x4 v0 = *(const f32x4*)(stg + rl * SSTR + cc * 8), v1 = *(const f32x4*)(stg + rl * SSTR + cc * 8 + 4);
    if constexpr (MODE == MODE_QKV) {
      const float qs = 0.25f * 1.4426950408889634f;
      v0 *= qs; v1 *= qs;
      u32x4 o = {pk2(v0[0], v0[1]), pk2(v0[2], v0[3]), pk2(v1[0], v1[1]), pk2(v1[2], v1[3])};
      *(u32x4*)(args.out0 + (size_t)m * D_ + cb * BN + cc * 8) = o;
    } else if constexpr (MODE == MODE_RES) {
      const size_t off = (size_t)m * D_ + cb * BN + cc * 8;
      const u32x4 rv = resv[pss];
      u32x4 o = {pk2(v0[0] + bflo(rv[0]), v0[1] + bfhi(rv[0])), pk2(v0[2] + bflo(rv[1]), v0[3] + bfhi(rv[1])),
                 pk2(v1[0] + bflo(rv[2]), v1[1] + bfhi(rv[2])), pk2(v1[2] + bflo(rv[3]), v1[3] + bfhi(rv[3]))};
      *(u32x4*)(args.out0 + off) = o;
      if constexpr (KTOT == 512) {
        float s1 = 0.f, s2 = 0.f;
#pragma unroll
        for (int j = 0; j < 4; ++j) { s1 = dot2bf(o[j], 0x3f803f80u, s1); s2 = dot2bf(o[j], o[j], s2); }
#pragma unroll
        for (int d = 1; d < CPR; d <<= 1) { s1 += __shfl_xor(s1, d); s2 += __shfl_xor(s2, d); }
        if (cc == 0) *(f32x2*)(args.hstat + ((size_t)cb * S_ + m) * 2) = (f32x2){s1, s2};
      }
    } else {
      const int f = cb * NC + cc * 8;
      if (f < F_) {
        u32x4 o = {pk2(v0[0], v0[1]), pk2(v0[2], v0[3]), pk2(v1[0], v1[1]), pk2(v1[2], v1[3])};
        *(u32x4*)(args.out0 + (size_t)m * F_ + f) = o;
      }
    }
  }
}

constexpr int F1_NST = 6, F1_STB = 8192;
__global__ __launch_bounds__(256) void ffn1x_kernel(const u16* __restrict__ hsrc, const u16* __restrict__ w1,
                                                    const u16* __restrict__ w3, const float* __restrict__ hstat,
                                                    const float* __restrict__ csum13, u16* __restrict__ act) {
  constexpr int SSTR = 36;
  __shared__ __attribute__((aligned(16))) char sm[F1_NST * F1_STB + 128 * SSTR * 4];
  float* stg = (float*)(sm + F1_NST * F1_STB);
  const int tid = threadIdx.x, lane = tid & 63, wid = tid >> 6, h = lane >> 5;
  const int row0 = blockIdx.x * 128;
  const int u0 = (blockIdx.y * 43) >> 4, u1 = ((blockIdx.y + 1) * 43) >> 4;
  const int nst = (u1 - u0) * 8;
  const u16* wp[2];
  int wrow[2];
#pragma unroll
  for (int i = 0; i < 2; ++i) {
    const int p = (i * 4 + wid) * 64 + lane, r = p >> 3, c = (p & 7) ^ wswz<64>(r);
    wrow[i] = r;
    wp[i] = ((c >> 2) ? w3 : w1) + u0 * 32 + (c & 3) * 8 + (size_t)r * F_;
  }
  const unsigned lds0 = (unsigned)(size_t)sm + (unsigned)__builtin_amdgcn_readfirstlane(wid) * 1024u;
  auto issue = [&](int s) {
    const unsigned base = lds0 + (s % F1_NST) * F1_STB;
    const size_t off = (size_t)(s & 7) * 64 * F_ + (s >> 3) * 32;
    glds16(wp[0] + off, base);
    glds16(wp[1] + off, base + 4096);
  };
#pragma unroll
  for (int s = 0; s < F1_NST - 1; ++s) issue(s);
  bf16x8 xf[32];
  {
    const u16* xr = hsrc + (size_t)(row0 + wid * 32 + (lane & 31)) * D_ + h * 8;
#pragma unroll
    for (int k = 0; k < 32; ++k) xf[k] = *(const bf16x8*)(xr + k * 16);
  }
  float mean, rstd;
  {
    f32x2 st = {0.f, 0.f};
#pragma unroll
    for (int j = 0; j < 8; ++j) st += *(const f32x2*)(hstat + ((size_t)j * S_ + row0 + wid * 32 + (lane & 31)) * 2);
    mean = st[0] * (1.f / 512);
    const float var = fmaxf(st[1] * (1.f / 512) - mean * mean, 0.f);
    rstd = 1.f / sqrtf(var + 1e-5f);
  }
  const unsigned tq = (lane & 15) >> 2, tp = lane & 3, tblk = (lane >> 4) & 1;
  for (int u = u0; u < u1; ++u) {
    f32x16 acc[2] = {};
    f32x4 c4[2][4];
#pragma unroll
    for (int tn = 0; tn < 2; ++tn)
#pragma unroll
      for (int g = 0; g < 4; ++g) c4[tn][g] = *(const f32x4*)(csum13 + tn * F_ + u * 32 + 8 * g + 4 * h);
#pragma unroll
    for (int kt = 0; kt < 8; ++kt) {
      const int s = (u - u0) * 8 + kt;
      const int rem = nst - 1 - s;
      if (rem >= F1_NST - 2) asm volatile("s_waitcnt vmcnt(%0)" ::"n"((F1_NST - 2) * 2) : "memory");
      else if (rem == 3) asm volatile("s_waitcnt vmcnt(6)" ::: "memory");
      else if (rem == 2) asm volatile("s_waitcnt vmcnt(4)" ::: "memory");
      else if (rem == 1) asm volatile("s_waitcnt vmcnt(2)" ::: "memory");
      else asm volatile("s_waitcnt vmcnt(0)" ::: "memory");
      __builtin_amdgcn_s_barrier();
      asm volatile("" ::: "memory");
      if (s + F1_NST - 1 < nst) issue(s + F1_NST - 1);
      const char* Ws = sm + (s % F1_NST) * F1_STB;
      bf16x8 wa[4][2];
#pragma unroll
      for (int ks = 0; ks < 4; ++ks)
#pragma unroll
        for (int t = 0; t < 2; ++t) {
          const unsigned c = t * 4 + 2 * tblk + (tp >> 1);
          const unsigned r0 = 16 * ks + 8 * h + tq, r1 = r0 + 4;
          s16x4 lo = __builtin_amdgcn_ds_read_tr16_b64_v4i16((lds_s16x4*)(Ws + r0 * 128 + ((c ^ wswz<64>(r0)) << 4) + 8 * (tp & 1)));
          s16x4 hi = __builtin_amdgcn_ds_read_tr16_b64_v4i16((lds_s16x4*)(Ws + r1 * 128 + ((c ^ wswz<64>(r1)) << 4) + 8 * (tp & 1)));
          wa[ks][t] = __builtin_shufflevector(lo, hi, 0, 1, 2, 3, 4, 5, 6, 7);
        }
#pragma unroll
      for (int ks = 0; ks < 4; ++ks) {
        acc[0] = MFMA32(wa[ks][0], xf[kt * 4 + ks], acc[0]);
        acc[1] = MFMA32(wa[ks][1], xf[kt * 4 + ks], acc[1]);
      }
    }
    float* srow = stg + (wid * 32 + (lane & 31)) * SSTR + 4 * h;
#pragma unroll
    for (int g = 0; g < 4; ++g) {
      f32x4 r;
#pragma unroll
      for (int e = 0; e < 4; ++e) {
        const float gv = rstd * (acc[0][4 * g + e] - mean * c4[0][g][e]);
        const float uv = rstd * (acc[1][4 * g + e] - mean * c4[1][g][e]);
        const unsigned gu = pk2(gv, uv);
        const float gt = bflo(gu), up = bfhi(gu);
        const float sg = gt * __builtin_amdgcn_rcpf(1.f + __builtin_amdgcn_exp2f(gt * -1.4426950408889634f));
        r[e] = bflo(pk2(sg, 0.f)) * up;
      }
      *(f32x4*)(srow + 8 * g) = r;
    }
    asm volatile("s_waitcnt lgkmcnt(0)" ::: "memory");
#pragma unroll
    for (int pss = 0; pss < 2; ++pss) {
      const int rl = wid * 32 + pss * 16 + (lane >> 2), cc = lane & 3;
      const f32x4 v0 = *(const f32x4*)(stg + rl * SSTR + cc * 8), v1 = *(const f32x4*)(stg + rl * SSTR + cc * 8 + 4);
      u32x4 o = {pk2(v0[0], v0[1]), pk2(v0[2], v0[3]), pk2(v1[0], v1[1]), pk2(v1[2], v1[3])};
      *(u32x4*)(act + (size_t)(row0 + rl) * F_ + u * 32 + cc * 8) = o;
    }
  }
}

template <bool EXACT>
DI void attn_step(f32x16& sc_, f32x16& sn_, f32x16& o, f32x16& negm, bf16x8& kn, const bf16x8 qf,
                  const char* kp, const char* vp0, int vstride, int vd, int t) {
  const bf16x8 v0 = *(const bf16x8*)(vp0 + t * vstride);
  const bf16x8 v1 = *(const bf16x8*)(vp0 + t * vstride + vd);
  sn_ = MFMA32(kn, qf, negm);
  kn = *(const bf16x8*)(kp + ((t + 2) & 31) * 1024);
  if constexpr (EXACT) {
    float m0 = fmaxf(fmaxf(sc_[0], sc_[1]), sc_[2]), m1 = fmaxf(fmaxf(sc_[3], sc_[4]), sc_[5]);
    float m2 = fmaxf(fmaxf(sc_[6], sc_[7]), sc_[8]), m3 = fmaxf(fmaxf(sc_[9], sc_[10]), sc_[11]);
    float m4 = fmaxf(fmaxf(sc_[12], sc_[13]), sc_[14]);
    float mx = fmaxf(fmaxf(fmaxf(m0, m1), sc_[15]), fmaxf(fmaxf(m2, m3), m4));
    if (__builtin_expect(__any(mx > 8.f), 0)) {
      mx = fmaxf(mx, __shfl_xor(mx, 32));
      mx = fmaxf(mx, 0.f);
      const float sc = __builtin_amdgcn_exp2f(-mx);
      o *= sc; negm -= mx; sc_ -= mx; sn_ -= mx;
    }
  }
  u32x4 p0, p1;
#pragma unroll
  for (int i = 0; i < 4; ++i) {
    p0[i] = pk2(__builtin_amdgcn_exp2f(sc_[2 * i]), __builtin_amdgcn_exp2f(sc_[2 * i + 1]));
    p1[i] = pk2(__builtin_amdgcn_exp2f(sc_[8 + 2 * i]), __builtin_amdgcn_exp2f(sc_[9 + 2 * i]));
  }
  o = MFMA32(v0, __builtin_bit_cast(bf16x8, p0), o);
  o = MFMA32(v1, __builtin_bit_cast(bf16x8, p1), o);
}

constexpr int ATT_LDS = 131072 + 64;
__global__ __launch_bounds__(1024) void attn_kernel(const u16* __restrict__ Q, const u16* __restrict__ Kf,
                                                    const u16* __restrict__ Vf, u16* __restrict__ O) {
  __shared__ __attribute__((aligned(16))) char sm[ATT_LDS];
  const int tid = threadIdx.x, lane = tid & 63, wid = tid >> 6;
  const int widu = __builtin_amdgcn_readfirstlane(wid);
  const int h = lane >> 5, r = lane & 31;
  const int q = blockIdx.x * 8 + (wid & 7), half = wid >> 3;
  bf16x8 qf;
  asm volatile("global_load_dwordx4 %0, %1, off" : "=v"(qf) : "v"(Q + (size_t)q * D_ + r * 16 + h * 8) : "memory");
  {
    const unsigned lds0 = (unsigned)(size_t)sm;
    const int hf = (widu >> 2) & 1, isv = widu >> 3, pc = widu & 3;
    const unsigned off0 = isv * 65536 + hf * 32768 + pc * 2048;
    const char* src = (const char*)(isv ? Vf : Kf) + hf * 32768 + pc * 2048 + lane * 16;
#pragma unroll
    for (int c = 0; c < 4; ++c) {
      glds16(src + c * 8192, lds0 + off0 + c * 8192);
      glds16(src + c * 8192 + 1024, lds0 + off0 + c * 8192 + 1024);
    }
    if (tid < 8) ((unsigned*)(sm + 131072))[tid] = tid < 4 ? 0x3f803f80u : 0u;
  }
  asm volatile("s_waitcnt vmcnt(6) lgkmcnt(0)" : "+v"(qf)::"memory");
  __builtin_amdgcn_s_barrier();
  asm volatile("" ::: "memory");
  const char* kp = sm + half * 32768 + r * 32 + h * 16;
  const char* vp0 = sm + (r < 16 ? 65536 + half * 32768 + h * 256 + r * 16 : (r == 16 ? 131072 : 131088));
  const int vd = r < 16 ? 512 : 0;
  const int vstride = r < 16 ? 1024 : 0;
  f32x16 negm, sA, sB = {};
  {
    f32x16 z = {};
    f32x16 s0 = MFMA32(*(const bf16x8*)kp, qf, z);
    float mx = s0[0];
#pragma unroll
    for (int i = 1; i < 16; ++i) mx = fmaxf(mx, s0[i]);
    mx = fmaxf(mx, __shfl_xor(mx, 32));
    negm = (f32x16)(-mx);
    sA = s0 + negm;
  }
  f32x16 o = {};
  bf16x8 kn = *(const bf16x8*)(kp + 1024);
#define ATT_SEG(T0, T1)                                                            \
  for (int t = T0; t < T1; t += 2) {                                               \
    attn_step<false>(sA, sB, o, negm, kn, qf, kp, vp0, vstride, vd, t);            \
    attn_step<false>(sB, sA, o, negm, kn, qf, kp, vp0, vstride, vd, t + 1);        \
  }
#define ATT_SYNC(N)                                                                \
  asm volatile("s_waitcnt vmcnt(" #N ")" ::: "memory");                            \
  __builtin_amdgcn_s_barrier();                                                    \
  asm volatile("" ::: "memory");
  ATT_SEG(0, 6)
  ATT_SYNC(4)
  ATT_SEG(6, 14)
  ATT_SYNC(2)
  ATT_SEG(14, 22)
  ATT_SYNC(0)
  ATT_SEG(22, 32)
  {
    const float l = __shfl(o[8], r);
    if (__any(!(l < 1e30f))) {
      o = (f32x16){};
      sA = MFMA32(*(const bf16x8*)kp, qf, negm);
      kn = *(const bf16x8*)(kp + 1024);
      for (int t = 0; t < 32; t += 2) {
        attn_step<true>(sA, sB, o, negm, kn, qf, kp, vp0, vstride, vd, t);
        attn_step<true>(sB, sA, o, negm, kn, qf, kp, vp0, vstride, vd, t + 1);
      }
    }
  }
  __syncthreads();
  float* ex = (float*)sm + (wid & 7) * (64 * 10);
  if (half == 1) {
#pragma unroll
    for (int i = 0; i < 9; ++i) ex[i * 64 + lane] = o[i];
    ex[9 * 64 + lane] = negm[0];
  }
  __syncthreads();
  if (half == 0) {
    const float nm1 = ex[9 * 64 + lane], nm0 = negm[0];
    const float nm = fminf(nm0, nm1);
    const float a0 = __builtin_amdgcn_exp2f(nm - nm0), a1 = __builtin_amdgcn_exp2f(nm - nm1);
#pragma unroll
    for (int i = 0; i < 9; ++i) o[i] = o[i] * a0 + ex[i * 64 + lane] * a1;
    const float l = __shfl(o[8], r);
    const float rl = 1.f / l;
    u32x2 oa = {pk2(o[0] * rl, o[1] * rl), pk2(o[2] * rl, o[3] * rl)};
    u32x2 ob = {pk2(o[4] * rl, o[5] * rl), pk2(o[6] * rl, o[7] * rl)};
    *(u32x2*)(O + (size_t)q * D_ + r * 16 + 4 * h) = oa;
    *(u32x2*)(O + (size_t)q * D_ + r * 16 + 8 + 4 * h) = ob;
  }
}

extern "C" void kernel_launch(void* const* d_in, const int* in_sizes, int n_in, void* d_out, int out_size,
                              void* d_ws, size_t ws_size, hipStream_t stream) {
  const u16* x = (const u16*)d_in[0];
  const u16* wq = (const u16*)d_in[1];
  const u16* wk = (const u16*)d_in[2];
  const u16* wv = (const u16*)d_in[3];
  const u16* wo = (const u16*)d_in[4];
  const u16* w1 = (const u16*)d_in[6];
  const u16* w3 = (const u16*)d_in[7];
  const u16* w2 = (const u16*)d_in[8];
  u16* out = (u16*)d_out;
  char* ws = (char*)d_ws;
  u16* Q = (u16*)(ws);
  u16* Kf = (u16*)(ws + (2u << 20));
  u16* Vf = (u16*)(ws + (2u << 20) + 65536);
  u16* O = (u16*)(ws + (3u << 20));
  u16* Hb = (u16*)(ws + (5u << 20));
  u16* ACT = (u16*)(ws + (7u << 20));
  float* hstat = (float*)(ws + (2u << 20) + 131072);
  float* csum13 = hstat + 16 * 4096;
  {
    GemmArgs a = {x, wq, wk, wv, wo, Q, Kf, Vf, hstat, csum13, nullptr, w1, w3, w2};
    gemm_kernel<MODE_QKV, 64, 64, 512, 4><<<dim3(32, 11), 256, 0, stream>>>(a);
  }
  attn_kernel<<<dim3(256), 1024, 0, stream>>>(Q, Kf, Vf, O);
  {
    GemmArgs a = {O, wo, nullptr, nullptr, x, Hb, nullptr, nullptr, hstat, nullptr, nullptr, nullptr, nullptr, nullptr};
    gemm_kernel<MODE_RES, 64, 64, 512, 4><<<dim3(32, 8), 256, 0, stream>>>(a);
  }
  {
    ffn1x_kernel<<<dim3(16, 16), 256, 0, stream>>>(Hb, w1, w3, hstat, csum13, ACT);
  }
  {
    GemmArgs a = {ACT, w2, nullptr, nullptr, Hb, out, nullptr, nullptr, nullptr, nullptr, nullptr, nullptr, nullptr, nullptr};
    gemm_kernel<MODE_RES, 64, 64, 1376, 4><<<dim3(32, 8), 256, 0, stream>>>(a);
  }
}
```

```cpp
#include <hip/hip_runtime.h>
#include <hip/hip_bf16.h>
#include <stdint.h>
#include <type_traits>


typedef short bf16x8 __attribute__((ext_vector_type(8)));
typedef short s16x4 __attribute__((ext_vector_type(4)));
typedef float f32x16 __attribute__((ext_vector_type(16)));
typedef float f32x4 __attribute__((ext_vector_type(4)));
typedef float f32x2 __attribute__((ext_vector_type(2)));
typedef __bf16 bf16x2v __attribute__((ext_vector_type(2)));
typedef unsigned u32x4 __attribute__((ext_vector_type(4)));
typedef unsigned u32x2 __attribute__((ext_vector_type(2)));
typedef __attribute__((address_space(3))) s16x4 lds_s16x4;
typedef unsigned short u16;

#define DI __device__ __forceinline__
#define MFMA32(a, b, c) __builtin_amdgcn_mfma_f32_32x32x16_bf16((a), (b), (c), 0, 0, 0)

constexpr int S_ = 2048, D_ = 512, F_ = 1376;

DI unsigned pk2(float a, float b) {
  f32x2 v = {a, b};
  bf16x2v r = __builtin_convertvector(v, bf16x2v);
  return __builtin_bit_cast(unsigned, r);
}
DI float bflo(unsigned u) { return __uint_as_float(u << 16); }
DI float bfhi(unsigned u) { return __uint_as_float(u & 0xffff0000u); }
DI float dot2bf(unsigned a, unsigned b, float c) {
  return __builtin_amdgcn_fdot2_f32_bf16(__builtin_bit_cast(bf16x2v, a), __builtin_bit_cast(bf16x2v, b), c, false);
}

DI void glds16(const void* gsrc, unsigned lds_dst) {
  unsigned keep;
  asm volatile("s_mov_b32 %0, m0\n\ts_mov_b32 m0, %2\n\ts_nop 0\n\tglobal_load_lds_dwordx4 %1, off\n\ts_mov_b32 m0, %0"
               : "=&s"(keep) : "v"(gsrc), "s"(lds_dst) : "memory");
}

enum { MODE_QKV = 0, MODE_RES = 1, MODE_FFN1 = 2 };

template <int BN>
DI unsigned wswz(unsigned row) {
  if constexpr (BN == 128) return ((row & 3) << 2) | ((row >> 2) & 3);
  else return ((row >> 1) & 1) << 2;
}

struct GemmArgs {
  const u16* A;
  const u16* W0;
  const u16* W1;
  const u16* W2;
  const u16* res;
  u16* out0;
  u16* out1;
  u16* out2;
  float* hstat;
  float* csum13;
  float* kmax;
  const u16* w1;
  const u16* w3;
  const u16* out2src;
};

template <int MODE, int BM, int BN, int KTOT, int NST, int DBG = 0, int WGM = 2, int NW = 4>
__global__ __launch_bounds__(NW * 64) void gemm_kernel(GemmArgs args) {
  constexpr int WGN = NW / WGM, WMT = BM / (32 * WGM), WNT = BN / (32 * WGN);
  constexpr int XB = BM * 128, WB = 64 * BN * 2, STB = XB + WB;
  constexpr int NXI = BM / (8 * NW), NWI = BN / (8 * NW), G = NXI + NWI;
  constexpr int NKT = (KTOT + 63) / 64;
  constexpr bool LN = (MODE != MODE_RES);
  constexpr bool LNSTAT = (MODE == MODE_QKV);
  constexpr int LDA = (MODE == MODE_RES) ? KTOT : 512;
  constexpr int LDW = (MODE == MODE_FFN1) ? F_ : 512;
  __shared__ __attribute__((aligned(16))) char sm[NST * STB];
  const int tid = threadIdx.x, lane = tid & 63, wid = tid >> 6;
  const int wm = wid % WGM, wn = wid / WGM, h = lane >> 5;
  const int row0 = blockIdx.x * BM, cb = blockIdx.y;
  if constexpr (MODE == MODE_QKV) {
    if (cb >= 9) {
      const int j = blockIdx.x + (cb - 9) * 32;
      if (j < 22) {
        const int cc = tid & 15, rg = tid >> 4;
        const int col = j * 128 + cc * 8;
        float a[8] = {};
        if (col < 2 * F_) {
          const u16* wsrc = (col < F_ ? args.w1 + col : args.w3 + (col - F_)) + (size_t)rg * 32 * F_;
          u32x4 v[32];
#pragma unroll
          for (int r = 0; r < 32; ++r) v[r] = *(const u32x4*)(wsrc + (size_t)r * F_);
#pragma unroll
          for (int r = 0; r < 32; ++r)
#pragma unroll
            for (int e = 0; e < 4; ++e) { a[2 * e] += bflo(v[r][e]); a[2 * e + 1] += bfhi(v[r][e]); }
        }
        float* red = (float*)sm;
#pragma unroll
        for (int e = 0; e < 8; ++e) red[rg * 128 + cc * 8 + e] = a[e];
        __syncthreads();
        if (tid < 128 && j * 128 + tid < 2 * F_) {
          float t = 0.f;
#pragma unroll
          for (int g = 0; g < 16; ++g) t += red[g * 128 + tid];
          args.csum13[j * 128 + tid] = t;
        }
      } else {
        const size_t base = ((size_t)(j - 22) * 16 * 256 + tid) * 16;
        u32x4 t[16];
#pragma unroll
        for (int i = 0; i < 16; ++i) {
          const size_t off = base + (size_t)i * 4096;
          const char* pp = off < 524288 ? (const char*)args.res + off
                         : (off < 524288 + (size_t)F_ * 1024 ? (const char*)args.out2src + (off - 524288) : (const char*)args.res);
          asm volatile("global_load_dwordx4 %0, %1, off" : "=v"(t[i]) : "v"(pp) : "memory");
        }
        asm volatile("s_waitcnt vmcnt(0)" ::: "memory");
#pragma unroll
        for (int i = 0; i < 16; ++i) asm volatile("" ::"v"(t[i]));
      }
      return;
    }
  }

  const u16* xp[NXI];
#pragma unroll
  for (int i = 0; i < NXI; ++i) {
    const int r = (i * NW + wid) * 8 + (lane >> 3), c = (lane & 7) ^ ((r >> 1) & 7);
    xp[i] = args.A + (size_t)(row0 + r) * LDA + c * 8;
  }
  const u16* wp[NWI];
  int wrow[NWI], wld[NWI];
#pragma unroll
  for (int i = 0; i < NWI; ++i) {
    const int p = (i * NW + wid) * 64 + lane;
    const int r = p / (BN / 8), c = (p % (BN / 8)) ^ wswz<BN>(r);
    wrow[i] = r;
    if constexpr (MODE == MODE_QKV) {
      if (cb < 8) { wp[i] = args.W0 + cb * 64 + c * 8; wld[i] = 512; }
      else if (c < 2) { wp[i] = args.W1 + c * 8; wld[i] = 16; }
      else { wp[i] = args.W2 + (c & 1) * 8; wld[i] = 16; }
    } else if constexpr (MODE == MODE_RES) {
      wp[i] = args.W0 + cb * BN + c * 8; wld[i] = 512;
    } else {
      const int c32 = c >> 2;
      int f = cb * (BN / 2) + (c32 >> 1) * 32 + (c & 3) * 8;
      if (f >= F_) f = 0;
      wp[i] = ((c32 & 1) ? args.W1 : args.W0) + f; wld[i] = F_;
    }
  }
  const unsigned lds0 = (unsigned)(size_t)sm + (unsigned)__builtin_amdgcn_readfirstlane(wid) * 1024u;
  auto issue = [&](int kt, int buf) {
    const unsigned base = lds0 + buf * STB;
#pragma unroll
    for (int i = 0; i < NXI; ++i) glds16(xp[i] + kt * 64, base + i * (NW * 1024));
#pragma unroll
    for (int i = 0; i < NWI; ++i) {
      int k = kt * 64 + wrow[i];
      if constexpr (KTOT % 64 != 0) k = k < KTOT ? k : KTOT - 1;
      glds16(wp[i] + (size_t)k * wld[i], base + XB + i * (NW * 1024));
    }
  };

  f32x16 acc[WNT][WMT] = {};
  float sx = 0.f, sxx = 0.f, cs = 0.f;
  const int wmu = __builtin_amdgcn_readfirstlane(wm), wnu = __builtin_amdgcn_readfirstlane(wn);
  unsigned xoff[WMT];
#pragma unroll
  for (int t = 0; t < WMT; ++t) xoff[t] = (unsigned)((wm * WMT + t) * 32 + (lane & 31)) * 128u;
  const unsigned xsw = ((lane & 31) >> 1) & 7;
  const unsigned tq = (lane & 15) >> 2, tp = lane & 3, tblk = (lane >> 4) & 1;

  constexpr int PD = NST - 1;
#pragma unroll
  for (int i = 0; i < PD; ++i)
    if (i < NKT) issue(i, i);
  constexpr int NC = (MODE == MODE_FFN1) ? BN / 2 : BN;
  constexpr int CPR = NC / 8;
  constexpr int RPP = NW * 64 / CPR;
  const int cc = tid % CPR, rr = tid / CPR;
  float mean[WMT], rstd[WMT];
  f32x4 c4[WNT][4];
  f32x2 hs[WMT][8];
  u32x4 resv[BM / RPP];
  if constexpr (MODE == MODE_FFN1) {
#pragma unroll
    for (int t = 0; t < WMT; ++t)
#pragma unroll
      for (int j = 0; j < 8; ++j) hs[t][j] = *(const f32x2*)(args.hstat + ((size_t)j * S_ + row0 + (wm * WMT + t) * 32 + (lane & 31)) * 2);
    const int fb = min(cb * (BN / 2) + wn * 32, F_ - 32);
#pragma unroll
    for (int tn = 0; tn < WNT; ++tn)
#pragma unroll
      for (int g = 0; g < 4; ++g) c4[tn][g] = *(const f32x4*)(args.csum13 + tn * F_ + fb + 8 * g + 4 * h);
  }
  if constexpr (MODE == MODE_RES) {
#pragma unroll
    for (int pss = 0; pss < BM / RPP; ++pss)
      resv[pss] = *(const u32x4*)(args.res + (size_t)(row0 + pss * RPP + rr) * D_ + cb * BN + cc * 8);
  }
  asm volatile("" ::: "memory");
  for (int kt = 0; kt < NKT; ++kt) {
    const int rem = NKT - 1 - kt;
    if (rem >= PD - 1) asm volatile("s_waitcnt vmcnt(%0)" ::"n"((PD - 1) * G) : "memory");
    else if (PD >= 3 && rem == 1) asm volatile("s_waitcnt vmcnt(%0)" ::"n"(G) : "memory");
    else if (PD >= 4 && rem == 2) asm volatile("s_waitcnt vmcnt(%0)" ::"n"(2 * G) : "memory");
    else asm volatile("s_waitcnt vmcnt(0)" ::: "memory");
    __builtin_amdgcn_s_barrier();
    asm volatile("" ::: "memory");
    if (kt + PD < NKT && DBG != 2) issue(kt + PD, (kt + PD) % NST);
    const char* Xs = sm + (kt % NST) * STB;
    const char* Ws = Xs + XB;
    const int nks = (KTOT % 64 != 0 && kt == NKT - 1) ? (KTOT % 64) / 16 : 4;
    if (DBG != 1) {
      bf16x8 xb[4][WMT], wa[4][WNT];
#pragma unroll
      for (int ks = 0; ks < 4; ++ks) {
        if (ks < nks) {
#pragma unroll
          for (int t = 0; t < WMT; ++t) xb[ks][t] = *(const bf16x8*)(Xs + xoff[t] + (((2 * ks + h) ^ xsw) << 4));
#pragma unroll
          for (int t = 0; t < WNT; ++t) {
            const unsigned c = (wn * WNT + t) * 4 + 2 * tblk + (tp >> 1);
            const unsigned r0 = 16 * ks + 8 * h + tq, r1 = r0 + 4;
            s16x4 lo = __builtin_amdgcn_ds_read_tr16_b64_v4i16((lds_s16x4*)(Ws + r0 * (BN * 2) + ((c ^ wswz<BN>(r0)) << 4) + 8 * (tp & 1)));
            s16x4 hi = __builtin_amdgcn_ds_read_tr16_b64_v4i16((lds_s16x4*)(Ws + r1 * (BN * 2) + ((c ^ wswz<BN>(r1)) << 4) + 8 * (tp & 1)));
            wa[ks][t] = __builtin_shufflevector(lo, hi, 0, 1, 2, 3, 4, 5, 6, 7);
          }
        }
      }
      __builtin_amdgcn_sched_barrier(0);
#pragma unroll
      for (int ks = 0; ks < 4; ++ks) {
        if (ks < nks) {
#pragma unroll
          for (int tn = 0; tn < WNT; ++tn)
#pragma unroll
            for (int tm = 0; tm < WMT; ++tm) acc[tn][tm] = MFMA32(wa[ks][tn], xb[ks][tm], acc[tn][tm]);
          if constexpr (LNSTAT) {
            static_assert(WMT == 1 && WNT == 1, "in-kernel LN stats assume a 64x64 tile");
            if (wmu == wnu) {
              const u32x4 u = __builtin_bit_cast(u32x4, xb[ks][0]);
#pragma unroll
              for (int j = 0; j < 4; ++j) { sx = dot2bf(u[j], 0x3f803f80u, sx); sxx = dot2bf(u[j], u[j], sxx); }
            } else {
              const u32x4 u = __builtin_bit_cast(u32x4, wa[ks][0]);
#pragma unroll
              for (int j = 0; j < 4; ++j) cs = dot2bf(u[j], 0x3f803f80u, cs);
            }
          }
        }
      }
    }
  }

  if constexpr (LNSTAT) {
    __syncthreads();
    float* csum_s = (float*)sm;
    float* stat_s = (float*)sm + BN;
    if (wmu == wnu) {
      const float s1 = sx + __shfl_xor(sx, 32), s2 = sxx + __shfl_xor(sxx, 32);
      const float mu = s1 * (1.f / 512);
      const float var = fmaxf(s2 * (1.f / 512) - mu * mu, 0.f);
      if (h == 0) *(f32x2*)(stat_s + (wm * 32 + lane) * 2) = (f32x2){mu, 1.f / sqrtf(var + 1e-5f)};
    } else {
      const float c = cs + __shfl_xor(cs, 32);
      if (h == 0) csum_s[wn * 32 + lane] = c;
    }
    __syncthreads();
    const f32x2 st = *(const f32x2*)(stat_s + (wm * 32 + (lane & 31)) * 2);
    mean[0] = st[0]; rstd[0] = st[1];
#pragma unroll
    for (int g = 0; g < 4; ++g) {
      const f32x4 c4 = *(const f32x4*)(csum_s + wn * 32 + 8 * g + 4 * h);
#pragma unroll
      for (int e = 0; e < 4; ++e) acc[0][0][4 * g + e] = rstd[0] * (acc[0][0][4 * g + e] - mean[0] * c4[e]);
    }
  } else if constexpr (LN) {
#pragma unroll
    for (int t = 0; t < WMT; ++t) {
      f32x2 st = hs[t][0];
#pragma unroll
      for (int j = 1; j < 8; ++j) st += hs[t][j];
      mean[t] = st[0] * (1.f / 512);
      const float var = fmaxf(st[1] * (1.f / 512) - mean[t] * mean[t], 0.f);
      rstd[t] = 1.f / sqrtf(var + 1e-5f);
    }
#pragma unroll
    for (int tn = 0; tn < WNT; ++tn)
#pragma unroll
      for (int g = 0; g < 4; ++g)
#pragma unroll
        for (int tm = 0; tm < WMT; ++tm)
#pragma unroll
          for (int e = 0; e < 4; ++e) acc[tn][tm][4 * g + e] = rstd[tm] * (acc[tn][tm][4 * g + e] - mean[tm] * c4[tn][g][e]);
  }

  if constexpr (MODE == MODE_QKV) {
    if (cb == 8) {
      if (wn == 0) {
        const int m = row0 + wm * 32 + (lane & 31);
        const f32x16& a = acc[0][0];
#pragma unroll
        for (int g = 0; g < 2; ++g) {
          u32x2 o = {pk2(a[4 * g], a[4 * g + 1]), pk2(a[4 * g + 2], a[4 * g + 3])};
          *(u32x2*)(args.out1 + (size_t)m * 16 + 8 * g + 4 * h) = o;
        }
        const int kt = m >> 5, ks = (m >> 4) & 1, ka = (m >> 3) & 1, kh = (m >> 2) & 1, kb = m & 3;
        u16* vbase = args.out2 + (size_t)((((kt * 2 + ks) * 2 + kh) * 16) * 8) + 4 * ka + kb;
#pragma unroll
        for (int g = 2; g < 4; ++g)
#pragma unroll
          for (int e = 0; e < 4; ++e) vbase[(8 * (g - 2) + 4 * h + e) * 8] = (u16)(pk2(a[4 * g + e], 0.f) & 0xffffu);
      }
      return;
    }
  }
  constexpr int SSTR = NC + 4;
  static_assert(BM * SSTR * 4 <= NST * STB, "staging tile must fit");
  float* stg = (float*)sm;
  __syncthreads();
#pragma unroll
  for (int tm = 0; tm < WMT; ++tm) {
    float* srow = stg + ((wm * WMT + tm) * 32 + (lane & 31)) * SSTR + 4 * h;
    if constexpr (MODE == MODE_FFN1) {
#pragma unroll
      for (int g = 0; g < 4; ++g) {
        f32x4 r;
#pragma unroll
        for (int e = 0; e < 4; ++e) {
          const unsigned gu = pk2(acc[0][tm][4 * g + e], acc[1][tm][4 * g + e]);
          const float gt = bflo(gu), up = bfhi(gu);
          const float sg = gt * __builtin_amdgcn_rcpf(1.f + __builtin_amdgcn_exp2f(gt * -1.4426950408889634f));
          r[e] = bflo(pk2(sg, 0.f)) * up;
        }
        *(f32x4*)(srow + wn * 32 + 8 * g) = r;
      }
    } else {
#pragma unroll
      for (int tn = 0; tn < WNT; ++tn)
#pragma unroll
        for (int g = 0; g < 4; ++g) {
          const f32x16& a = acc[tn][tm];
          *(f32x4*)(srow + (wn * WNT + tn) * 32 + 8 * g) = (f32x4){a[4 * g], a[4 * g + 1], a[4 * g + 2], a[4 * g + 3]};
        }
    }
  }
  __syncthreads();
#pragma unroll
  for (int pss = 0; pss < BM / RPP; ++pss) {
    const int rl = pss * RPP + rr, m = row0 + rl;
    f32x4 v0 = *(const f32x4*)(stg + rl * SSTR + cc * 8), v1 = *(const f32x4*)(stg + rl * SSTR + cc * 8 + 4);
    if constexpr (MODE == MODE_QKV) {
      const float qs = 0.25f * 1.4426950408889634f;
      v0 *= qs; v1 *= qs;
      u32x4 o = {pk2(v0[0], v0[1]), pk2(v0[2], v0[3]), pk2(v1[0], v1[1]), pk2(v1[2], v1[3])};
      *(u32x4*)(args.out0 + (size_t)m * D_ + cb * BN + cc * 8) = o;
    } else if constexpr (MODE == MODE_RES) {
      const size_t off = (size_t)m * D_ + cb * BN + cc * 8;
      const u32x4 rv = resv[pss];
      u32x4 o = {pk2(v0[0] + bflo(rv[0]), v0[1] + bfhi(rv[0])), pk2(v0[2] + bflo(rv[1]), v0[3] + bfhi(rv[1])),
                 pk2(v1[0] + bflo(rv[2]), v1[1] + bfhi(rv[2])), pk2(v1[2] + bflo(rv[3]), v1[3] + bfhi(rv[3]))};
      *(u32x4*)(args.out0 + off) = o;
      if constexpr (KTOT == 512) {
        float s1 = 0.f, s2 = 0.f;
#pragma unroll
        for (int j = 0; j < 4; ++j) { s1 = dot2bf(o[j], 0x3f803f80u, s1); s2 = dot2bf(o[j], o[j], s2); }
#pragma unroll
        for (int d = 1; d < CPR; d <<= 1) { s1 += __shfl_xor(s1, d); s2 += __shfl_xor(s2, d); }
        if (cc == 0) *(f32x2*)(args.hstat + ((size_t)cb * S_ + m) * 2) = (f32x2){s1, s2};
      }
    } else {
      const int f = cb * NC + cc * 8;
      if (f < F_) {
        u32x4 o = {pk2(v0[0], v0[1]), pk2(v0[2], v0[3]), pk2(v1[0], v1[1]), pk2(v1[2], v1[3])};
        *(u32x4*)(args.out0 + (size_t)m * F_ + f) = o;
      }
    }
  }
}

template <bool EXACT>
DI void attn_step(f32x16& sc_, f32x16& sn_, f32x16& o, f32x16& negm, bf16x8& kn, const bf16x8 qf,
                  const char* kp, const char* vp0, int vstride, int vd, int t) {
  const bf16x8 v0 = *(const bf16x8*)(vp0 + t * vstride);
  const bf16x8 v1 = *(const bf16x8*)(vp0 + t * vstride + vd);
  sn_ = MFMA32(kn, qf, negm);
  kn = *(const bf16x8*)(kp + ((t + 2) & 31) * 1024);
  if constexpr (EXACT) {
    float m0 = fmaxf(fmaxf(sc_[0], sc_[1]), sc_[2]), m1 = fmaxf(fmaxf(sc_[3], sc_[4]), sc_[5]);
    float m2 = fmaxf(fmaxf(sc_[6], sc_[7]), sc_[8]), m3 = fmaxf(fmaxf(sc_[9], sc_[10]), sc_[11]);
    float m4 = fmaxf(fmaxf(sc_[12], sc_[13]), sc_[14]);
    float mx = fmaxf(fmaxf(fmaxf(m0, m1), sc_[15]), fmaxf(fmaxf(m2, m3), m4));
    if (__builtin_expect(__any(mx > 8.f), 0)) {
      mx = fmaxf(mx, __shfl_xor(mx, 32));
      mx = fmaxf(mx, 0.f);
      const float sc = __builtin_amdgcn_exp2f(-mx);
      o *= sc; negm -= mx; sc_ -= mx; sn_ -= mx;
    }
  }
  u32x4 p0, p1;
#pragma unroll
  for (int i = 0; i < 4; ++i) {
    p0[i] = pk2(__builtin_amdgcn_exp2f(sc_[2 * i]), __builtin_amdgcn_exp2f(sc_[2 * i + 1]));
    p1[i] = pk2(__builtin_amdgcn_exp2f(sc_[8 + 2 * i]), __builtin_amdgcn_exp2f(sc_[9 + 2 * i]));
  }
  o = MFMA32(v0, __builtin_bit_cast(bf16x8, p0), o);
  o = MFMA32(v1, __builtin_bit_cast(bf16x8, p1), o);
}

constexpr int ATT_LDS = 131072 + 64;
__global__ __launch_bounds__(1024) void attn_kernel(const u16* __restrict__ Q, const u16* __restrict__ Kf,
                                                    const u16* __restrict__ Vf, u16* __restrict__ O) {
  __shared__ __attribute__((aligned(16))) char sm[ATT_LDS];
  const int tid = threadIdx.x, lane = tid & 63, wid = tid >> 6;
  const int widu = __builtin_amdgcn_readfirstlane(wid);
  const int h = lane >> 5, r = lane & 31;
  const int q = blockIdx.x * 8 + (wid & 7), half = wid >> 3;
  bf16x8 qf;
  asm volatile("global_load_dwordx4 %0, %1, off" : "=v"(qf) : "v"(Q + (size_t)q * D_ + r * 16 + h * 8) : "memory");
  {
    const unsigned lds0 = (unsigned)(size_t)sm;
    const int hf = (widu >> 2) & 1, isv = widu >> 3, pc = widu & 3;
    const unsigned off0 = isv * 65536 + hf * 32768 + pc * 2048;
    const char* src = (const char*)(isv ? Vf : Kf) + hf * 32768 + pc * 2048 + lane * 16;
#pragma unroll
    for (int c = 0; c < 4; ++c) {
      glds16(src + c * 8192, lds0 + off0 + c * 8192);
      glds16(src + c * 8192 + 1024, lds0 + off0 + c * 8192 + 1024);
    }
    if (tid < 8) ((unsigned*)(sm + 131072))[tid] = tid < 4 ? 0x3f803f80u : 0u;
  }
  asm volatile("s_waitcnt vmcnt(6) lgkmcnt(0)" : "+v"(qf)::"memory");
  __builtin_amdgcn_s_barrier();
  asm volatile("" ::: "memory");
  const char* kp = sm + half * 32768 + r * 32 + h * 16;
  const char* vp0 = sm + (r < 16 ? 65536 + half * 32768 + h * 256 + r * 16 : (r == 16 ? 131072 : 131088));
  const int vd = r < 16 ? 512 : 0;
  const int vstride = r < 16 ? 1024 : 0;
  f32x16 negm, sA, sB = {};
  {
    f32x16 z = {};
    f32x16 s0 = MFMA32(*(const bf16x8*)kp, qf, z);
    float mx = s0[0];
#pragma unroll
    for (int i = 1; i < 16; ++i) mx = fmaxf(mx, s0[i]);
    mx = fmaxf(mx, __shfl_xor(mx, 32));
    negm = (f32x16)(-mx);
    sA = s0 + negm;
  }
  f32x16 o = {};
  bf16x8 kn = *(const bf16x8*)(kp + 1024);
#define ATT_SEG(T0, T1)                                                            \
  for (int t = T0; t < T1; t += 2) {                                               \
    attn_step<false>(sA, sB, o, negm, kn, qf, kp, vp0, vstride, vd, t);            \
    attn_step<false>(sB, sA, o, negm, kn, qf, kp, vp0, vstride, vd, t + 1);        \
  }
#define ATT_SYNC(N)                                                                \
  asm volatile("s_waitcnt vmcnt(" #N ")" ::: "memory");                            \
  __builtin_amdgcn_s_barrier();                                                    \
  asm volatile("" ::: "memory");
  ATT_SEG(0, 6)
  ATT_SYNC(4)
  ATT_SEG(6, 14)
  ATT_SYNC(2)
  ATT_SEG(14, 22)
  ATT_SYNC(0)
  ATT_SEG(22, 32)
  {
    const float l = __shfl(o[8], r);
    if (__any(!(l < 1e30f))) {
      o = (f32x16){};
      sA = MFMA32(*(const bf16x8*)kp, qf, negm);
      kn = *(const bf16x8*)(kp + 1024);
      for (int t = 0; t < 32; t += 2) {
        attn_step<true>(sA, sB, o, negm, kn, qf, kp, vp0, vstride, vd, t);
        attn_step<true>(sB, sA, o, negm, kn, qf, kp, vp0, vstride, vd, t + 1);
      }
    }
  }
  __syncthreads();
  float* ex = (float*)sm + (wid & 7) * (64 * 10);
  if (half == 1) {
#pragma unroll
    for (int i = 0; i < 9; ++i) ex[i * 64 + lane] = o[i];
    ex[9 * 64 + lane] = negm[0];
  }
  __syncthreads();
  if (half == 0) {
    const float nm1 = ex[9 * 64 + lane], nm0 = negm[0];
    const float nm = fminf(nm0, nm1);
    const float a0 = __builtin_amdgcn_exp2f(nm - nm0), a1 = __builtin_amdgcn_exp2f(nm - nm1);
#pragma unroll
    for (int i = 0; i < 9; ++i) o[i] = o[i] * a0 + ex[i * 64 + lane] * a1;
    const float l = __shfl(o[8], r);
    const float rl = 1.f / l;
    u32x2 oa = {pk2(o[0] * rl, o[1] * rl), pk2(o[2] * rl, o[3] * rl)};
    u32x2 ob = {pk2(o[4] * rl, o[5] * rl), pk2(o[6] * rl, o[7] * rl)};
    *(u32x2*)(O + (size_t)q * D_ + r * 16 + 4 * h) = oa;
    *(u32x2*)(O + (size_t)q * D_ + r * 16 + 8 + 4 * h) = ob;
  }
}

extern "C" void kernel_launch(void* const* d_in, const int* in_sizes, int n_in, void* d_out, int out_size,
                              void* d_ws, size_t ws_size, hipStream_t stream) {
  const u16* x = (const u16*)d_in[0];
  const u16* wq = (const u16*)d_in[1];
  const u16* wk = (const u16*)d_in[2];
  const u16* wv = (const u16*)d_in[3];
  const u16* wo = (const u16*)d_in[4];
  const u16* w1 = (const u16*)d_in[6];
  const u16* w3 = (const u16*)d_in[7];
  const u16* w2 = (const u16*)d_in[8];
  u16* out = (u16*)d_out;
  char* ws = (char*)d_ws;
  u16* Q = (u16*)(ws);
  u16* Kf = (u16*)(ws + (2u << 20));
  u16* Vf = (u16*)(ws + (2u << 20) + 65536);
  u16* O = (u16*)(ws + (3u << 20));
  u16* Hb = (u16*)(ws + (5u << 20));
  u16* ACT = (u16*)(ws + (7u << 20));
  float* hstat = (float*)(ws + (2u << 20) + 131072);
  float* csum13 = hstat + 16 * 4096;
  {
    GemmArgs a = {x, wq, wk, wv, wo, Q, Kf, Vf, hstat, csum13, nullptr, w1, w3, w2};
    gemm_kernel<MODE_QKV, 64, 64, 512, 3><<<dim3(32, 11), 256, 0, stream>>>(a);
  }
  attn_kernel<<<dim3(256), 1024, 0, stream>>>(Q, Kf, Vf, O);
  {
    GemmArgs a = {O, wo, nullptr, nullptr, x, Hb, nullptr, nullptr, hstat, nullptr, nullptr, nullptr, nullptr, nullptr};
    gemm_kernel<MODE_RES, 64, 64, 512, 4><<<dim3(32, 8), 256, 0, stream>>>(a);
  }
  {
    GemmArgs a = {Hb, w1, w3, nullptr, nullptr, ACT, nullptr, nullptr, hstat, csum13, nullptr, nullptr, nullptr, nullptr};
    gemm_kernel<MODE_FFN1, 128, 64, 512, 2, 0, 4><<<dim3(16, 43), 256, 0, stream>>>(a);
  }
  {
    GemmArgs a = {ACT, w2, nullptr, nullptr, Hb, out, nullptr, nullptr, nullptr, nullptr, nullptr, nullptr, nullptr, nullptr};
    gemm_kernel<MODE_RES, 64, 64, 1376, 4><<<dim3(32, 8), 256, 0, stream>>>(a);
  }
}
```
